```python
import torch

def _softmax(x, axis=-1):
    return torch.softmax(x, dim=axis)
def _gelu(x):
    return torch.nn.functional.gelu(x, approximate="tanh")
def _silu(x):
    return torch.nn.functional.silu(x)
def _sigmoid(x):
    return torch.sigmoid(x)
def _layernorm(x, axis=-1, eps=1e-5):
    m = x.mean(dim=axis, keepdim=True)
    v = x.var(dim=axis, keepdim=True, unbiased=False)
    return (x - m) / torch.sqrt(v + eps)
def _scatter_add(tgt, idx, src, axis=0):
    return tgt.clone().index_add_(axis, idx, src)
def _segment_sum(src, seg, n):
    out = torch.zeros((n,) + src.shape[1:], dtype=src.dtype)
    return out.index_add_(0, seg, src)
def _pad(x, pw):
    flat = tuple(v for pair in reversed(pw) for v in pair)
    return torch.nn.functional.pad(x, flat)

kernel_name = 'gated_mix__sliding_block_cached__swiglu_ffn__decode'

def setup_inputs():

    _w = (lambda a: (lambda t: t.to(torch.bfloat16) if t.is_floating_point() else t)(torch.as_tensor(a)))
    def _A_setup():
        B, Sq, D = 2, 1, 512
        Hq, Hkv, Sk, Dh = 32, 8, 16384, 128
        assert Hq % Hkv == 0, (Hq, Hkv)
        return {
            "x": _w((lambda *s: torch.randn(*s, dtype=torch.bfloat16) * 0.05)(B, Sq, D)),
            "k_cache": _w((lambda *s: torch.randn(*s, dtype=torch.bfloat16) * 6.4)(B, Hkv, Sk, Dh)),
            "v_cache": _w((lambda *s: torch.randn(*s, dtype=torch.bfloat16) * 0.4)(B, Hkv, Sk, Dh)),
            "wq": _w((lambda *s: torch.randn(*s, dtype=torch.bfloat16) * 6.4)(D, Hq * Dh) * (D ** -0.5)),
            "wo": _w((lambda *s: torch.randn(*s, dtype=torch.bfloat16) * 0.4)(Hq * Dh, D) * ((Hq * Dh) ** -0.5)),
        }
    _A_in = _A_setup()

    def _B_setup():
        B, S, D, F = 2, 1, 512, 1792
        return {
            "x": _w((lambda *s: torch.randn(*s, dtype=torch.bfloat16) * 0.05)(B, S, D)),
            "w_gate": _w((lambda *s: torch.randn(*s, dtype=torch.bfloat16) * 0.05)(D, F)),
            "w_up": _w((lambda *s: torch.randn(*s, dtype=torch.bfloat16) * 0.05)(D, F)),
            "w_down": _w((lambda *s: torch.randn(*s, dtype=torch.bfloat16) * 0.05)(F, D)),
        }
    _B_in = _B_setup()

    return {
        "A_x": _A_in["x"],
        "A_k_cache": _A_in["k_cache"],
        "A_v_cache": _A_in["v_cache"],
        "A_wq": _A_in["wq"],
        "A_wo": _A_in["wo"],
        "B_x": _B_in["x"],
        "B_w_gate": _B_in["w_gate"],
        "B_w_up": _B_in["w_up"],
        "B_w_down": _B_in["w_down"],
        "gate_w": _w((lambda *s: torch.randn(*s, dtype=torch.bfloat16) * 0.05)(512) * 16.0),
    }

def reference(A_x, A_k_cache, A_v_cache, A_wq, A_wo, B_x, B_w_gate, B_w_up, B_w_down, gate_w):
    def _A_ref(x, k_cache, v_cache, wq, wo):
        B, Sq, D = x.shape
        Hq, Dh = 32, 128
        Hkv, Sk = k_cache.shape[1], k_cache.shape[2]
        W = min(4096, max(256, Sk // 4))
        rep = Hq // Hkv
        q = (lambda x, *d: x.permute(*d))((x @ wq).reshape(B, Sq, Hq, Dh), 0, 2, 1, 3)
        k = (lambda x, n, axis: torch.repeat_interleave(x, n, dim=axis))(k_cache[:, :, Sk - W:, :], rep, axis=1)
        v = (lambda x, n, axis: torch.repeat_interleave(x, n, dim=axis))(v_cache[:, :, Sk - W:, :], rep, axis=1)
        scores = torch.einsum("bhqd,bhkd->bhqk", q, k) * (Dh ** -0.5)
        p = _softmax(scores, axis=-1)
        o = torch.einsum("bhqk,bhkd->bhqd", p, v)
        return (lambda x, *d: x.permute(*d))(o, 0, 2, 1, 3).reshape(B, Sq, Hq * Dh) @ wo

    def _B_ref(x, w_gate, w_up, w_down):
        g = _silu(x @ w_gate)
        u = x @ w_up
        return (g * u) @ w_down

    x = A_x
    g = _sigmoid((x * gate_w).sum(-1, keepdims=True))
    a = _A_ref(x, A_k_cache, A_v_cache, A_wq, A_wo)
    b = _B_ref(x, B_w_gate, B_w_up, B_w_down)
    return g * a + (1 - g) * b
```

```cpp
#include <hip/hip_runtime.h>
#include <hip/hip_bf16.h>
typedef unsigned short u16;
__device__ __forceinline__ float bf2f(u16 v){ return __uint_as_float(((unsigned)v)<<16); }
__device__ __forceinline__ float rbf(float f){ return __bfloat162float(__float2bfloat16(f)); }
constexpr int B=2,D=512,HQ=32,HKV=8,SK=16384,DH=128,W=4096,F=1792,NS=16,KPS=W/NS;

__global__ void k1(const u16* x,const u16* wq,const u16* wg,const u16* wu,float* q,float* h){
  int c=blockIdx.x*64+threadIdx.x;
  if(c<4096){ float a0=0,a1=0; for(int k=0;k<D;k++){ float w=bf2f(wq[k*4096+c]); a0+=bf2f(x[k])*w; a1+=bf2f(x[D+k])*w;} q[c]=rbf(a0); q[4096+c]=rbf(a1);}
  else { c-=4096; if(c<F){ float g0=0,g1=0,u0=0,u1=0; for(int k=0;k<D;k++){ float a=bf2f(wg[k*F+c]),b=bf2f(wu[k*F+c]); float x0=bf2f(x[k]),x1=bf2f(x[D+k]); g0+=x0*a; g1+=x1*a; u0+=x0*b; u1+=x1*b;}
    g0=rbf(g0);g1=rbf(g1);u0=rbf(u0);u1=rbf(u1);
    float s0=rbf(g0/(1.f+__expf(-g0))), s1=rbf(g1/(1.f+__expf(-g1)));
    h[c]=rbf(s0*u0); h[F+c]=rbf(s1*u1);} }
}
__global__ void k2(const u16* kc,const u16* vc,const float* q,float* po,float* pml){
  int s=blockIdx.x, kvh=blockIdx.y, b=blockIdx.z, t=threadIdx.x;
  __shared__ float qs[4][128]; __shared__ float p[4][256]; __shared__ float red[4][256];
  for(int i=t;i<512;i+=256) qs[i>>7][i&127]=q[b*4096+kvh*512+i];
  __syncthreads();
  const u16* kr=kc+((size_t)(b*HKV+kvh)*SK+(SK-W)+s*KPS+t)*DH;
  float sc[4]={0,0,0,0};
  for(int d=0;d<DH;d++){ float kv=bf2f(kr[d]); for(int g=0;g<4;g++) sc[g]+=qs[g][d]*kv; }
  for(int g=0;g<4;g++){ sc[g]*=0.08838834764831845f; red[g][t]=sc[g]; }
  __syncthreads();
  float m[4]; for(int g=0;g<4;g++){ float mm=-1e30f; for(int i=0;i<256;i++) mm=fmaxf(mm,red[g][i]); m[g]=mm; p[g][t]=__expf(sc[g]-mm);}
  __syncthreads();
  for(int o=t;o<512;o+=256){ int g=o>>7,d=o&127; const u16* vr=vc+((size_t)(b*HKV+kvh)*SK+(SK-W)+s*KPS)*DH+d; float a=0,l=0; for(int i=0;i<256;i++){ a+=p[g][i]*bf2f(vr[(size_t)i*DH]); l+=p[g][i]; }
    int row=(b*HQ+kvh*4+g); po[(row*NS+s)*DH+d]=a; if(d==0){ pml[(row*NS+s)*2]=m[g]; pml[(row*NS+s)*2+1]=l; } }
}
__global__ void k3(const float* po,const float* pml,float* o){
  int row=blockIdx.x,d=threadIdx.x; float M=-1e30f; for(int s=0;s<NS;s++) M=fmaxf(M,pml[(row*NS+s)*2]);
  float L=0,a=0; for(int s=0;s<NS;s++){ float w=__expf(pml[(row*NS+s)*2]-M); L+=w*pml[(row*NS+s)*2+1]; a+=w*po[(row*NS+s)*DH+d]; }
  o[row*DH+d]=rbf(a/L);
}
__global__ void k4(const u16* x,const u16* gw,const u16* wo,const u16* wd,const float* o,const float* h,__hip_bfloat16* out){
  int b=blockIdx.y,c=blockIdx.x*64+threadIdx.x; float a=0,f=0,z=0;
  for(int k=0;k<4096;k++) a+=o[b*4096+k]*bf2f(wo[k*D+c]);
  for(int k=0;k<F;k++) f+=h[b*F+k]*bf2f(wd[k*D+c]);
  for(int k=0;k<D;k++) z+=rbf(bf2f(x[b*D+k])*bf2f(gw[k]));
  z=rbf(z); float g=rbf(1.f/(1.f+__expf(-z))); a=rbf(a); f=rbf(f);
  out[b*D+c]=__float2bfloat16(rbf(g*a)+rbf(rbf(1.f-g)*f));
}
extern "C" void kernel_launch(void* const* d_in,const int* in_sizes,int n_in,void* d_out,int out_size,void* d_ws,size_t ws_size,hipStream_t stream){
  const u16* x=(const u16*)d_in[0]; const u16* kc=(const u16*)d_in[1]; const u16* vc=(const u16*)d_in[2];
  const u16* wq=(const u16*)d_in[3]; const u16* wo=(const u16*)d_in[4];
  const u16* wg=(const u16*)d_in[6]; const u16* wu=(const u16*)d_in[7]; const u16* wd=(const u16*)d_in[8]; const u16* gw=(const u16*)d_in[9];
  float* ws=(float*)d_ws; float* q=ws; float* h=ws+8192; float* o=ws+16384; float* pml=ws+32768; float* po=ws+65536;
  k1<<<(4096+F)/64,64,0,stream>>>(x,wq,wg,wu,q,h);
  k2<<<dim3(NS,HKV,B),256,0,stream>>>(kc,vc,q,po,pml);
  k3<<<B*HQ,128,0,stream>>>(po,pml,o);
  k4<<<dim3(D/64,B),64,0,stream>>>(x,gw,wo,wd,o,h,(__hip_bfloat16*)d_out);
}
```

```cpp
#include <hip/hip_runtime.h>
#include <hip/hip_bf16.h>

typedef unsigned short u16;
typedef unsigned u32;
typedef unsigned long long u64;
typedef u32 u32x4 __attribute__((ext_vector_type(4)));
typedef u32 u32x2 __attribute__((ext_vector_type(2)));
typedef u32 u32x8 __attribute__((ext_vector_type(8)));
typedef u32 u32x16 __attribute__((ext_vector_type(16)));
typedef float f32x4 __attribute__((ext_vector_type(4)));
typedef short s16x4 __attribute__((ext_vector_type(4)));
typedef __bf16 bf16x8 __attribute__((ext_vector_type(8)));
typedef __bf16 bf16x2 __attribute__((ext_vector_type(2)));
#define LAS __attribute__((address_space(3)))
#define GAS __attribute__((address_space(1)))
#define CAS __attribute__((address_space(4)))

#define STAMP_DECL
#define STAMP(i)
#define STAMP_FLUSH
#define XSH 4
#define PSLEEP 1

namespace {
constexpr int D = 512, HKV = 8, SK = 16384, DH = 128, WIN = 4096, F = 1792;
constexpr u32 WS_QP = 4096;
constexpr u32 WS_HP = WS_QP + 8 * 2 * 4 * 4096;
constexpr u32 WS_OP = WS_HP + 32 * 2 * 4096;
constexpr u32 WS_FP = WS_OP + 64 * 32 * 384;
constexpr u32 WS_END = WS_FP + 64 * 8192;
constexpr u32 L_T0 = 0, L_T1 = 16384, L_WO = 32768  , L_OW = L_WO + 24576  , OW_STRIDE = 4 * 528;
constexpr u32 L_Q = L_OW + 8 * OW_STRIDE  , L_M = L_Q + 4 * 272  , L_L = L_M + 128,
              L_OS = L_L + 128  , L_H = L_OS + 32 * 144  , L_W = L_H + 128  ,
              L_G = L_W + 128, L_END = L_G + 16;
constexpr int SPIN_CAP = 1 << 16;

__device__ __forceinline__ f32x4 mfma(u32x4 a, u32x4 b, f32x4 c) {
  return __builtin_amdgcn_mfma_f32_16x16x32_bf16(__builtin_bit_cast(bf16x8, a), __builtin_bit_cast(bf16x8, b), c, 0, 0, 0);
}
__device__ __forceinline__ u32 pk(float lo, float hi) {
  bf16x2 v = {(__bf16)lo, (__bf16)hi};
  return __builtin_bit_cast(u32, v);
}
__device__ __forceinline__ float rbf(float f) { return (float)(__bf16)f; }
__device__ __forceinline__ float blo(u32 v) { return __uint_as_float(v << 16); }
__device__ __forceinline__ float bhi(u32 v) { return __uint_as_float(v & 0xffff0000u); }
template <int CTRL> __device__ __forceinline__ float dpp(float x) {
  return __builtin_bit_cast(float, __builtin_amdgcn_mov_dpp(__builtin_bit_cast(int, x), CTRL, 0xf, 0xf, true));
}
template <int CTRL> __device__ __forceinline__ u32 dppu(u32 x) { return (u32)__builtin_amdgcn_mov_dpp((int)x, CTRL, 0xf, 0xf, true); }
constexpr int ROR1 = 0x121, ROR2 = 0x122, ROR4 = 0x124, ROR8 = 0x128, SHL4 = 0x104, SHL8 = 0x108, XOR1 = 0xB1;
__device__ __forceinline__ float row_sum(float x) { x += dpp<ROR8>(x); x += dpp<ROR4>(x); x += dpp<ROR2>(x); x += dpp<ROR1>(x); return x; }
__device__ __forceinline__ float row_max(float x) {
  x = fmaxf(x, dpp<ROR8>(x)); x = fmaxf(x, dpp<ROR4>(x)); x = fmaxf(x, dpp<ROR2>(x)); x = fmaxf(x, dpp<ROR1>(x)); return x;
}
__device__ __forceinline__ float x16_sum(float x) {
  auto s = __builtin_amdgcn_permlane16_swap(__float_as_uint(x), __float_as_uint(x), false, false);
  return __uint_as_float(s[0]) + __uint_as_float(s[1]);
}
__device__ __forceinline__ float x32_sum(float x) {
  auto s = __builtin_amdgcn_permlane32_swap(__float_as_uint(x), __float_as_uint(x), false, false);
  return __uint_as_float(s[0]) + __uint_as_float(s[1]);
}
__device__ __forceinline__ float x16_32_max(float x) {
  auto s = __builtin_amdgcn_permlane16_swap(__float_as_uint(x), __float_as_uint(x), false, false);
  x = fmaxf(__uint_as_float(s[0]), __uint_as_float(s[1]));
  auto t = __builtin_amdgcn_permlane32_swap(__float_as_uint(x), __float_as_uint(x), false, false);
  return fmaxf(__uint_as_float(t[0]), __uint_as_float(t[1]));
}
__device__ __forceinline__ void bar() { asm volatile("s_waitcnt lgkmcnt(0)\n\ts_barrier" ::: "memory"); }
__device__ __forceinline__ void bar0() { asm volatile("s_barrier" ::: "memory"); }
__device__ __forceinline__ void put(__amdgpu_buffer_rsrc_t ws, u32 off, u32 a, u32 b, u32 tag) {
  __builtin_amdgcn_raw_buffer_store_b128(u32x4{a, tag, b, tag}, ws, off, 0, 16);
}
__device__ __forceinline__ u32x4 get(__amdgpu_buffer_rsrc_t ws, u32 off) { return __builtin_amdgcn_raw_buffer_load_b128(ws, off, 0, 16); }
__device__ __forceinline__ bool fresh(u32x4 v, u32 tag) { return (v.y == tag) & (v.w == tag); }
template <int N> __device__ __forceinline__ void gather(__amdgpu_buffer_rsrc_t ws, u32 off, u32 stride, u32 tag, u32x4 (&v)[N], bool need = true) {
#pragma unroll
  for (int k = 0; k < N; ++k) v[k] = u32x4{0, 0, 0, 0};
#pragma unroll 1
  for (int spin = 0; spin < SPIN_CAP; ++spin) {
    asm volatile("" ::: "memory");
    if (need) {
      bool ok = true;
#pragma unroll
      for (int k = 0; k < N; ++k) v[k] = get(ws, off + k * stride);
#pragma unroll
      for (int k = 0; k < N; ++k) ok &= fresh(v[k], tag);
      need = !ok;
    }
    if (!__any(need)) break;
    __builtin_amdgcn_s_sleep(PSLEEP);
  }
}
__device__ __forceinline__ void dma2(const char* base, u32 va, u32 vb, u32 lds) {
  asm volatile("s_mov_b32 m0, %2\n\ts_nop 0\n\tglobal_load_lds_dwordx4 %0, %3 nt\n\ts_add_u32 m0, m0, 0x400\n\ts_nop 0\n\tglobal_load_lds_dwordx4 %1, %3 nt"
               :: "v"(va), "v"(vb), "s"(lds), "s"(base) : "memory", "scc");
}
__device__ __forceinline__ u32x2 trd(LAS char* p) {
  return __builtin_bit_cast(u32x2, __builtin_amdgcn_ds_read_tr16_b64_v4i16((LAS s16x4*)p));
}
__device__ __forceinline__ u32x4 trd2(LAS char* p, int d) { const u32x2 lo = trd(p), hi = trd(p + d); return u32x4{lo.x, lo.y, hi.x, hi.y}; }
__device__ __forceinline__ void proj_put(LAS char* tile, const u32x4 (&xf)[4], int t, int lane, __amdgpu_buffer_rsrc_t ws, u32 off, u32 bstride, u32 tag) {
  const int g = lane >> 4, q = (lane >> 2) & 3, p = lane & 3, f = (q >> 1) | ((g & 1) << 1);
  LAS char* a = tile + (8 * g + q) * 128 + ((t ^ f) * 32) + p * 8;
  const f32x4 z = {0.f, 0.f, 0.f, 0.f};
  f32x4 c0 = mfma(trd2(a, 512), xf[0], z), c1 = mfma(trd2(a + 4096, 512), xf[1], z);
  c0 = mfma(trd2(a + 8192, 512), xf[2], c0);
  c1 = mfma(trd2(a + 12288, 512), xf[3], c1);
  c0 += c1;
  const bool up = lane & 2;
  if ((lane & 12) == 0)
    put(ws, off + (lane & 1) * bstride + (g * 2 + ((lane >> 1) & 1)) * 16, __float_as_uint(up ? c0[2] : c0[0]), __float_as_uint(up ? c0[3] : c0[1]), tag);
}
}

struct Args { const void *x, *kc, *vc, *wq, *wo, *wg, *wu, *wd, *gw; void *out, *ws, *ws2; };

__global__ void __launch_bounds__(768, 3) fused_decode(Args) {
  __shared__ __attribute__((aligned(1024))) char smem[L_END];
  LAS char* L = (LAS char*)smem;
  const u32 lbase = (u32)(size_t)L;
  STAMP_DECL;
  STAMP(0);
  u32x16 ka; u32x8 kb;
  asm volatile("s_load_dwordx16 %0, %2, 0x0\n\ts_load_dwordx8 %1, %2, 0x40\n\ts_waitcnt lgkmcnt(0)"
               : "=&s"(ka), "=&s"(kb) : "s"(__builtin_amdgcn_kernarg_segment_ptr()));
#define PA(i) ((const char*)(((u64)ka[2 * (i) + 1] << 32) | ka[2 * (i)]))
#define PB(i) ((char*)(((u64)kb[2 * (i) + 1] << 32) | kb[2 * (i)]))
  const char *x = PA(0), *kc = PA(1), *vc = PA(2), *wq = PA(3), *wo = PA(4), *wg = PA(5), *wu = PA(6), *wd = PA(7), *gw = PB(0);
  char *out = PB(1), *wsp = PB(2);
  const int j = blockIdx.x, xg = j & 7, i = j >> 3, xs = (xg + XSH) & 7;
  const int wave = __builtin_amdgcn_readfirstlane(threadIdx.x >> 6), lane = threadIdx.x & 63;
  const int g = lane >> 4, n = lane & 15;
  const int b = i & 1, s = i >> 1, c = i & 3;
  const __amdgpu_buffer_rsrc_t ws = __builtin_amdgcn_make_buffer_rsrc(wsp, 0, WS_END, 0x00020000);
  const f32x4 z4 = {0.f, 0.f, 0.f, 0.f};
#define LOAD_TAG() (*(const CAS u32*)wsp * 1664525u + 1013904223u)

  if (wave >= 4) {
    const int w = wave - 4;
    const size_t kvoff = ((size_t)(b * HKV + xg) * SK + (SK - WIN) + s * 256 + ((w + s + 3 * xg + 4 * b) & 7) * 32) * (DH * 2) + 4096;
    const GAS char* kp = (const GAS char*)kc + kvoff;
    const GAS char* vp = (const GAS char*)vc + kvoff;
    const u32 ko = n * 256 + g * 16, vo = g * 1024 + n * 16;
    u32x4 K[8], V[8];
    if (w >= 4) bar0();
#pragma unroll
    for (int u = 0; u < 8; ++u) K[u] = __builtin_nontemporal_load((const GAS u32x4*)(kp + ko + ((u >> 2) - 1) * 4096 + (u & 3) * 64));
    __builtin_amdgcn_sched_barrier(0);
    if (w < 4) bar0();
    bar0();
#pragma unroll
    for (int u = 0; u < 8; ++u) V[u] = __builtin_nontemporal_load((const GAS u32x4*)(vp + vo + ((u >> 2) - 1) * 4096 + (u & 3) * 256));
    __builtin_amdgcn_sched_barrier(0);
    const u32 tag = LOAD_TAG();
    STAMP(1);
    bar0();
    STAMP(2);
    f32x4 S0 = z4, S1 = z4;
#pragma unroll
    for (int ks = 0; ks < 4; ++ks) {
      const u32x4 qf = *(LAS u32x4*)(L + L_Q + (n & 3) * 272 + ks * 64 + g * 16);
      S0 = mfma(K[ks], qf, S0);
      S1 = mfma(K[4 + ks], qf, S1);
    }
    constexpr float C2 = 0.08838834764831845f * 1.4426950408889634f;
    float m = fmaxf(fmaxf(fmaxf(S0[0], S0[1]), fmaxf(S0[2], S0[3])), fmaxf(fmaxf(S1[0], S1[1]), fmaxf(S1[2], S1[3])));
    m = x16_32_max(m);
    if (lane < 4) *(LAS float*)(L + L_M + lane * 32 + w * 4) = m;
    bar();
    {
      const f32x4 ma = *(LAS f32x4*)(L + L_M + (n & 3) * 32), mb = *(LAS f32x4*)(L + L_M + (n & 3) * 32 + 16);
      m = fmaxf(fmaxf(fmaxf(ma[0], ma[1]), fmaxf(ma[2], ma[3])), fmaxf(fmaxf(mb[0], mb[1]), fmaxf(mb[2], mb[3]))) * C2;
    }
    u32x4 P;
    P.x = pk(__builtin_amdgcn_exp2f(fmaf(S0[0], C2, -m)), __builtin_amdgcn_exp2f(fmaf(S0[1], C2, -m)));
    P.y = pk(__builtin_amdgcn_exp2f(fmaf(S0[2], C2, -m)), __builtin_amdgcn_exp2f(fmaf(S0[3], C2, -m)));
    P.z = pk(__builtin_amdgcn_exp2f(fmaf(S1[0], C2, -m)), __builtin_amdgcn_exp2f(fmaf(S1[1], C2, -m)));
    P.w = pk(__builtin_amdgcn_exp2f(fmaf(S1[2], C2, -m)), __builtin_amdgcn_exp2f(fmaf(S1[3], C2, -m)));
    const f32x4 ls = mfma(u32x4{0x3f803f80u, 0x3f803f80u, 0x3f803f80u, 0x3f803f80u}, P, z4);
    if (lane < 4) *(LAS float*)(L + L_L + lane * 32 + w * 4) = ls[0];
    STAMP(3);
    f32x4 O[8];
#pragma unroll
    for (int d = 0; d < 8; ++d) {
      const u32 sel = (d & 1) ? 0x07060302u : 0x05040100u;
      u32x4 a;
#pragma unroll
      for (int e = 0; e < 4; ++e) a[e] = __builtin_amdgcn_perm(V[2 * e + 1][d >> 1], V[2 * e][d >> 1], sel);
      O[d] = mfma(a, P, z4);
    }
    if (n < 4) {
#pragma unroll
      for (int d = 0; d < 8; ++d) *(LAS f32x4*)(L + L_OW + w * OW_STRIDE + n * 528 + g * 128 + d * 16) = O[d];
    }
    STAMP(4);
    bar();
    {
      const int h = w >> 1;
      LAS char* src = L + L_OW + h * 528 + (w & 1) * 256 + lane * 4;
      float r[8];
#pragma unroll
      for (int u = 0; u < 8; ++u) r[u] = *(LAS float*)(src + u * OW_STRIDE);
      const f32x4 la = *(LAS f32x4*)(L + L_L + h * 32), lb = *(LAS f32x4*)(L + L_L + h * 32 + 16);
      const float v = ((r[0] + r[1]) + (r[2] + r[3])) + ((r[4] + r[5]) + (r[6] + r[7]));
      const float lsum = ((la[0] + la[1]) + (la[2] + la[3])) + ((lb[0] + lb[1]) + (lb[2] + lb[3]));
      const u32 p01 = pk(v, dpp<SHL4>(v)), p23 = dppu<SHL8>(p01);
      const u32 mh = __builtin_amdgcn_readlane(__float_as_uint(m), h);
      const bool dat = (lane & 12) == 0;
      const u32 slot = dat ? (lane >> 5) * 8 + (lane & 3) * 2 + (g & 1) : 16 + (lane & 3) + (g & 1) * 4;
      if (dat || ((lane & 44) == 4))
        put(ws, WS_OP + ((xg * 8 + w) * 32 + b * 16 + s) * 384 + slot * 16, dat ? p01 : mh, dat ? p23 : __float_as_uint(lsum), tag);
    }
    STAMP(5);
    bar0();
    const int r = xs * 8 + (i >> 2);
    {
      u32x4 v[1];
      gather<1>(ws, WS_OP + (r * 32 + w * 4 + g) * 384 + n * 16, 0, tag, v);
      STAMP(6);
      *(LAS u32x2*)(L + L_OS + (w * 4 + g) * 144 + ((n >> 3) * 32 + (n & 3) * 8 + ((n >> 2) & 1) * 4) * 2) = u32x2{v[0].x, v[0].z};
    }
    bar();
    STAMP(7);
    {
      const int q = (lane >> 2) & 3, p = lane & 3;
      LAS char* a = L + L_WO + (4 * g + q) * 256 + ((w ^ (4 * (g & 1) + q)) * 32) + p * 8;
      LAS char* os = L + L_OS + n * 144 + g * 16;
      const u32x4 w0 = trd2(a, 4096), w1 = trd2(a + 8192, 4096), w2 = trd2(a + 16384, 4096);
      f32x4 a0 = mfma(*(LAS u32x4*)os, w0, z4), a1 = mfma(*(LAS u32x4*)(os + 16 * 144), w0, z4);
      a0 = mfma(*(LAS u32x4*)(os + 64), w1, a0);
      a1 = mfma(*(LAS u32x4*)(os + 16 * 144 + 64), w1, a1);
      const f32x4 ff = mfma(*(LAS u32x4*)(L + L_H + (n & 1) * 64 + g * 16), w2, z4);
      const f32x4 s0 = *(LAS f32x4*)(L + L_W + g * 16), s1 = *(LAS f32x4*)(L + L_W + 64 + g * 16), gg = *(LAS f32x4*)(L + L_G);
      float r0 = x32_sum(x16_sum((a0[0] * s0[0] + a0[1] * s0[1]) + (a0[2] * s0[2] + a0[3] * s0[3]))) * gg[0];
      float r1 = x32_sum(x16_sum((a1[0] * s1[0] + a1[1] * s1[1]) + (a1[2] * s1[2] + a1[3] * s1[3]))) * gg[2];
      if ((i >> 3) * 8 + xs < 28) { r0 = fmaf(gg[1], ff[0], r0); r1 = fmaf(gg[3], ff[1], r1); }
      const float rr = (g & 1) ? r1 : r0, rn = dpp<XOR1>(rr);
      if ((lane & 33) == 0) put(ws, WS_FP + ((c * 8 + w) * 2 + g) * 8192 + r * 128 + n * 8, __float_as_uint(rr), __float_as_uint(rn), tag);
    }
    STAMP(8);
  } else {
    const u32 vrow128 = wave * 32 + (lane >> 3), vch128 = ((lane & 7) ^ (2 * (g & 1))) * 16;
    {
      const char* base = wq + (size_t)c * 128 * 8192 + (xs * 512 + (i >> 2) * 64) * 2;
      const u32 va = vrow128 * 8192 + vch128, vb = (va ^ 64) + 8 * 8192;
      dma2(base, va, vb, lbase + L_T0 + wave * 4096);
      dma2(base, va + 16 * 8192, vb + 16 * 8192, lbase + L_T0 + wave * 4096 + 2048);
    }
    u32x4 xf[4];
#pragma unroll
    for (int ks = 0; ks < 4; ++ks) xf[ks] = *(const GAS u32x4*)((const GAS char*)x + (n & 1) * 1024 + c * 256 + ks * 64 + g * 16);
    const u32 tag = LOAD_TAG();
    asm volatile("s_waitcnt vmcnt(0)" : "+v"(xf[0]), "+v"(xf[1]), "+v"(xf[2]), "+v"(xf[3]) :: "memory");
    STAMP(1);
    bar0();
    proj_put(L + L_T0, xf, wave, lane, ws, WS_QP + (xs * 8 + c) * 4096 + ((i >> 2) * 32 + wave * 8) * 16, 16384, tag);
    STAMP(2);
    if (wave == 3) {
      const u32x4 x0 = *(const GAS u32x4*)((const GAS char*)x + lane * 16), x1 = *(const GAS u32x4*)((const GAS char*)x + 1024 + lane * 16),
                  gv = *(const GAS u32x4*)((const GAS char*)gw + lane * 16);
      float z0 = 0.f, z1 = 0.f;
#pragma unroll
      for (int e = 0; e < 4; ++e) {
        z0 += rbf(blo(x0[e]) * blo(gv[e])) + rbf(bhi(x0[e]) * bhi(gv[e]));
        z1 += rbf(blo(x1[e]) * blo(gv[e])) + rbf(bhi(x1[e]) * bhi(gv[e]));
      }
      z0 = x32_sum(x16_sum(row_sum(z0))); z1 = x32_sum(x16_sum(row_sum(z1)));
      const float z = rbf((lane & 1) ? z1 : z0);
      const float sg = rbf(__builtin_amdgcn_rcpf(1.f + __expf(-z)));
      if (lane < 2) *(LAS u32x2*)(L + L_G + lane * 8) = u32x2{__float_as_uint(sg), __float_as_uint(rbf(1.f - sg))};
    }
    bar0();
    {
      u32x4 v[4];
      gather<4>(ws, WS_QP + (xg * 2 + b) * 16384 + (wave * 64 + lane) * 16, 4096, tag, v);
      const float q0 = (__uint_as_float(v[0].x) + __uint_as_float(v[1].x)) + (__uint_as_float(v[2].x) + __uint_as_float(v[3].x));
      const float q1 = (__uint_as_float(v[0].z) + __uint_as_float(v[1].z)) + (__uint_as_float(v[2].z) + __uint_as_float(v[3].z));
      *(LAS u32*)(L + L_Q + wave * 272 + lane * 4) = pk(q0, q1);
    }
    STAMP(3);
    bar();
    bar0();
    const int gp = (i >> 3) * 8 + xg, gc = (i >> 3) * 8 + xs;
    if (gp < 28) {
      const char* base = ((i & 4) ? wu : wg) + (size_t)c * 128 * (F * 2) + gp * 128;
      const u32 va = vrow128 * (F * 2) + vch128, vb = (va ^ 64) + 8 * F * 2;
      dma2(base, va, vb, lbase + L_T1 + wave * 4096);
      dma2(base, va + 16 * F * 2, vb + 16 * F * 2, lbase + L_T1 + wave * 4096 + 2048);
    }
    {
      const u32 va = g * 1024 + (n ^ (2 * g)) * 16, vb = (va ^ 128) + 4096;
      const char* base = wo + (size_t)(xs * 512 + (i >> 2) * 64 + wave * 16) * 1024 + c * 256;
      dma2(base, va, vb, lbase + L_WO + wave * 4096);
      dma2(base, va + 8192, vb + 8192, lbase + L_WO + wave * 4096 + 2048);
      const int row = gc < 28 ? gc * 64 + ((i >> 2) & 1) * 32 + wave * 8 : 0;
      dma2(wd + (size_t)row * 1024 + c * 256, va, vb, lbase + L_WO + 16384 + wave * 2048);
    }
    STAMP(4);
    bar0();
    asm volatile("s_waitcnt vmcnt(6)" ::: "memory");
    STAMP(5);
    bar0();
    if (gp < 28) proj_put(L + L_T1, xf, wave, lane, ws, WS_HP + (gp * 2 + (wave >> 1)) * 4096 + (i & 7) * 512 + (wave & 1) * 128, 256, tag);
    STAMP(6);
    if (wave == 1) {
      if (gc < 28) {
        u32x4 v[4];
        gather<4>(ws, WS_HP + (gc * 2 + ((i >> 2) & 1)) * 4096 + lane * 16, 1024, tag, v);
        const float g0 = rbf(x32_sum(__uint_as_float(v[0].x) + __uint_as_float(v[1].x))), g1 = rbf(x32_sum(__uint_as_float(v[0].z) + __uint_as_float(v[1].z)));
        const float u0 = rbf(x32_sum(__uint_as_float(v[2].x) + __uint_as_float(v[3].x))), u1 = rbf(x32_sum(__uint_as_float(v[2].z) + __uint_as_float(v[3].z)));
        const float h0 = rbf(g0 * __builtin_amdgcn_rcpf(1.f + __expf(-g0))) * u0, h1 = rbf(g1 * __builtin_amdgcn_rcpf(1.f + __expf(-g1))) * u1;
        if (lane < 32) *(LAS u32*)(L + L_H + g * 64 + (((n >> 1) & 3) * 8 + (n >> 3) * 4 + (n & 1) * 2) * 2) = pk(h0, h1);
      }
    } else if (wave == 2) {
      u32x4 v[1];
      gather<1>(ws, WS_OP + ((xs * 8 + (i >> 2)) * 32 + (lane & 31)) * 384 + 256, 0, tag, v, lane < 32);
      const float ms = __uint_as_float(v[0].x), M = row_max(ms), e = __builtin_amdgcn_exp2f(ms - M);
      const float Ls = row_sum(e * __uint_as_float(v[0].z));
      if (lane < 32) *(LAS float*)(L + L_W + lane * 4) = e / Ls;
    }
    asm volatile("s_waitcnt vmcnt(0)" ::: "memory");
    STAMP(7);
    bar();
    if (wave == 3 && xg < 2) {
      const int f = xg * 32 + i;
      u32x4 v[8];
      gather<8>(ws, WS_FP + f * 8192 + lane * 16, 1024, tag, v);
      STAMP(8);
      float r0 = 0.f, r1 = 0.f;
#pragma unroll
      for (int u = 0; u < 8; ++u) { r0 += __uint_as_float(v[u].x); r1 += __uint_as_float(v[u].z); }
      r0 += dpp<ROR8>(r0); r1 += dpp<ROR8>(r1);
      r0 = x32_sum(x16_sum(r0)); r1 = x32_sum(x16_sum(r1));
      if (lane < 8) ((GAS u32*)out)[(f & 1) * 256 + (f >> 1) * 8 + lane] = pk(r0, r1);
      if (f == 0 && lane == 0) __builtin_amdgcn_raw_buffer_store_b32(tag, ws, 0, 0, 16);
      STAMP(9);
    }
  }
  STAMP_FLUSH;
}

extern "C" void kernel_launch(void* const* d_in, const int* in_sizes, int n_in, void* d_out, int out_size, void* d_ws, size_t ws_size,
                              hipStream_t stream) {
  Args a{d_in[0], d_in[1], d_in[2], d_in[3], d_in[4], d_in[6], d_in[7], d_in[8], d_in[9], d_out, d_ws, d_ws};
  fused_decode<<<256, 768, 0, stream>>>(a);
}
```

```cpp
#define STAMP_DECL unsigned long long ts_[12]={0,0,0,0,0,0,0,0,0,0,0,0}
#define STAMP(i) asm volatile("s_memrealtime %0\n\ts_waitcnt lgkmcnt(0)":"=s"(ts_[i])::"memory")
#define STAMP_FLUSH do{ if(__builtin_amdgcn_readfirstlane(threadIdx.x>>6)==0){ unsigned long long x_= ts_[0]? ts_[0]-ts_[0] : 0; unsigned long long T_=ts_[0]+4000+8*x_; while(__builtin_amdgcn_s_memrealtime()<T_) __builtin_amdgcn_s_sleep(1);} }while(0)
#include <hip/hip_runtime.h>
#include <hip/hip_bf16.h>

typedef unsigned short u16;
typedef unsigned u32;
typedef unsigned long long u64;
typedef u32 u32x4 __attribute__((ext_vector_type(4)));
typedef u32 u32x2 __attribute__((ext_vector_type(2)));
typedef u32 u32x8 __attribute__((ext_vector_type(8)));
typedef u32 u32x16 __attribute__((ext_vector_type(16)));
typedef float f32x4 __attribute__((ext_vector_type(4)));
typedef short s16x4 __attribute__((ext_vector_type(4)));
typedef __bf16 bf16x8 __attribute__((ext_vector_type(8)));
typedef __bf16 bf16x2 __attribute__((ext_vector_type(2)));
#define LAS __attribute__((address_space(3)))
#define GAS __attribute__((address_space(1)))
#define CAS __attribute__((address_space(4)))

#define XSH 4
#define PSLEEP 1

namespace {
constexpr int D = 512, HKV = 8, SK = 16384, DH = 128, WIN = 4096, F = 1792;
constexpr u32 WS_QP = 4096;
constexpr u32 WS_HP = WS_QP + 8 * 2 * 4 * 4096;
constexpr u32 WS_OP = WS_HP + 32 * 2 * 4096;
constexpr u32 WS_FP = WS_OP + 64 * 32 * 384;
constexpr u32 WS_END = WS_FP + 64 * 8192;
constexpr u32 L_T0 = 0, L_T1 = 16384, L_WO = 32768  , L_OW = L_WO + 24576  , OW_STRIDE = 4 * 528;
constexpr u32 L_Q = L_OW + 8 * OW_STRIDE  , L_M = L_Q + 4 * 272  , L_L = L_M + 128,
              L_OS = L_L + 128  , L_H = L_OS + 32 * 144  , L_W = L_H + 128  ,
              L_G = L_W + 128, L_END = L_G + 16;
constexpr int SPIN_CAP = 1 << 16;

__device__ __forceinline__ f32x4 mfma(u32x4 a, u32x4 b, f32x4 c) {
  return __builtin_amdgcn_mfma_f32_16x16x32_bf16(__builtin_bit_cast(bf16x8, a), __builtin_bit_cast(bf16x8, b), c, 0, 0, 0);
}
__device__ __forceinline__ u32 pk(float lo, float hi) {
  bf16x2 v = {(__bf16)lo, (__bf16)hi};
  return __builtin_bit_cast(u32, v);
}
__device__ __forceinline__ float rbf(float f) { return (float)(__bf16)f; }
__device__ __forceinline__ float blo(u32 v) { return __uint_as_float(v << 16); }
__device__ __forceinline__ float bhi(u32 v) { return __uint_as_float(v & 0xffff0000u); }
template <int CTRL> __device__ __forceinline__ float dpp(float x) {
  return __builtin_bit_cast(float, __builtin_amdgcn_mov_dpp(__builtin_bit_cast(int, x), CTRL, 0xf, 0xf, true));
}
template <int CTRL> __device__ __forceinline__ u32 dppu(u32 x) { return (u32)__builtin_amdgcn_mov_dpp((int)x, CTRL, 0xf, 0xf, true); }
constexpr int ROR1 = 0x121, ROR2 = 0x122, ROR4 = 0x124, ROR8 = 0x128, SHL4 = 0x104, SHL8 = 0x108, XOR1 = 0xB1;
__device__ __forceinline__ float row_sum(float x) { x += dpp<ROR8>(x); x += dpp<ROR4>(x); x += dpp<ROR2>(x); x += dpp<ROR1>(x); return x; }
__device__ __forceinline__ float row_max(float x) {
  x = fmaxf(x, dpp<ROR8>(x)); x = fmaxf(x, dpp<ROR4>(x)); x = fmaxf(x, dpp<ROR2>(x)); x = fmaxf(x, dpp<ROR1>(x)); return x;
}
__device__ __forceinline__ float x16_sum(float x) {
  auto s = __builtin_amdgcn_permlane16_swap(__float_as_uint(x), __float_as_uint(x), false, false);
  return __uint_as_float(s[0]) + __uint_as_float(s[1]);
}
__device__ __forceinline__ float x32_sum(float x) {
  auto s = __builtin_amdgcn_permlane32_swap(__float_as_uint(x), __float_as_uint(x), false, false);
  return __uint_as_float(s[0]) + __uint_as_float(s[1]);
}
__device__ __forceinline__ float x16_32_max(float x) {
  auto s = __builtin_amdgcn_permlane16_swap(__float_as_uint(x), __float_as_uint(x), false, false);
  x = fmaxf(__uint_as_float(s[0]), __uint_as_float(s[1]));
  auto t = __builtin_amdgcn_permlane32_swap(__float_as_uint(x), __float_as_uint(x), false, false);
  return fmaxf(__uint_as_float(t[0]), __uint_as_float(t[1]));
}
__device__ __forceinline__ void bar() { asm volatile("s_waitcnt lgkmcnt(0)\n\ts_barrier" ::: "memory"); }
__device__ __forceinline__ void bar0() { asm volatile("s_barrier" ::: "memory"); }
__device__ __forceinline__ void put(__amdgpu_buffer_rsrc_t ws, u32 off, u32 a, u32 b, u32 tag) {
  __builtin_amdgcn_raw_buffer_store_b128(u32x4{a, tag, b, tag}, ws, off, 0, 16);
}
__device__ __forceinline__ u32x4 get(__amdgpu_buffer_rsrc_t ws, u32 off) { return __builtin_amdgcn_raw_buffer_load_b128(ws, off, 0, 16); }
__device__ __forceinline__ bool fresh(u32x4 v, u32 tag) { return (v.y == tag) & (v.w == tag); }
template <int N> __device__ __forceinline__ void gather(__amdgpu_buffer_rsrc_t ws, u32 off, u32 stride, u32 tag, u32x4 (&v)[N], bool need = true) {
#pragma unroll
  for (int k = 0; k < N; ++k) v[k] = u32x4{0, 0, 0, 0};
#pragma unroll 1
  for (int spin = 0; spin < SPIN_CAP; ++spin) {
    asm volatile("" ::: "memory");
    if (need) {
      bool ok = true;
#pragma unroll
      for (int k = 0; k < N; ++k) v[k] = get(ws, off + k * stride);
#pragma unroll
      for (int k = 0; k < N; ++k) ok &= fresh(v[k], tag);
      need = !ok;
    }
    if (!__any(need)) break;
    __builtin_amdgcn_s_sleep(PSLEEP);
  }
}
__device__ __forceinline__ void dma2(const char* base, u32 va, u32 vb, u32 lds) {
  asm volatile("s_mov_b32 m0, %2\n\ts_nop 0\n\tglobal_load_lds_dwordx4 %0, %3 nt\n\ts_add_u32 m0, m0, 0x400\n\ts_nop 0\n\tglobal_load_lds_dwordx4 %1, %3 nt"
               :: "v"(va), "v"(vb), "s"(lds), "s"(base) : "memory", "scc");
}
__device__ __forceinline__ u32x2 trd(LAS char* p) {
  return __builtin_bit_cast(u32x2, __builtin_amdgcn_ds_read_tr16_b64_v4i16((LAS s16x4*)p));
}
__device__ __forceinline__ u32x4 trd2(LAS char* p, int d) { const u32x2 lo = trd(p), hi = trd(p + d); return u32x4{lo.x, lo.y, hi.x, hi.y}; }
__device__ __forceinline__ void proj_put(LAS char* tile, const u32x4 (&xf)[4], int t, int lane, __amdgpu_buffer_rsrc_t ws, u32 off, u32 bstride, u32 tag) {
  const int g = lane >> 4, q = (lane >> 2) & 3, p = lane & 3, f = (q >> 1) | ((g & 1) << 1);
  LAS char* a = tile + (8 * g + q) * 128 + ((t ^ f) * 32) + p * 8;
  const f32x4 z = {0.f, 0.f, 0.f, 0.f};
  f32x4 c0 = mfma(trd2(a, 512), xf[0], z), c1 = mfma(trd2(a + 4096, 512), xf[1], z);
  c0 = mfma(trd2(a + 8192, 512), xf[2], c0);
  c1 = mfma(trd2(a + 12288, 512), xf[3], c1);
  c0 += c1;
  const bool up = lane & 2;
  if ((lane & 12) == 0)
    put(ws, off + (lane & 1) * bstride + (g * 2 + ((lane >> 1) & 1)) * 16, __float_as_uint(up ? c0[2] : c0[0]), __float_as_uint(up ? c0[3] : c0[1]), tag);
}
}

struct Args { const void *x, *kc, *vc, *wq, *wo, *wg, *wu, *wd, *gw; void *out, *ws, *ws2; };

__global__ void __launch_bounds__(768, 3) fused_decode(Args) {
  __shared__ __attribute__((aligned(1024))) char smem[L_END];
  LAS char* L = (LAS char*)smem;
  const u32 lbase = (u32)(size_t)L;
  STAMP_DECL;
  STAMP(0);
  u32x16 ka; u32x8 kb;
  asm volatile("s_load_dwordx16 %0, %2, 0x0\n\ts_load_dwordx8 %1, %2, 0x40\n\ts_waitcnt lgkmcnt(0)"
               : "=&s"(ka), "=&s"(kb) : "s"(__builtin_amdgcn_kernarg_segment_ptr()));
  STAMP(10);
#define PA(i) ((const char*)(((u64)ka[2 * (i) + 1] << 32) | ka[2 * (i)]))
#define PB(i) ((char*)(((u64)kb[2 * (i) + 1] << 32) | kb[2 * (i)]))
  const char *x = PA(0), *kc = PA(1), *vc = PA(2), *wq = PA(3), *wo = PA(4), *wg = PA(5), *wu = PA(6), *wd = PA(7), *gw = PB(0);
  char *out = PB(1), *wsp = PB(2);
  const int j = blockIdx.x, xg = j & 7, i = j >> 3, xs = (xg + XSH) & 7;
  const int wave = __builtin_amdgcn_readfirstlane(threadIdx.x >> 6), lane = threadIdx.x & 63;
  const int g = lane >> 4, n = lane & 15;
  const int b = i & 1, s = i >> 1, c = i & 3;
  const __amdgpu_buffer_rsrc_t ws = __builtin_amdgcn_make_buffer_rsrc(wsp, 0, WS_END, 0x00020000);
  const f32x4 z4 = {0.f, 0.f, 0.f, 0.f};
#define LOAD_TAG() (*(const CAS u32*)wsp * 1664525u + 1013904223u)

  if (wave >= 4) {
    const int w = wave - 4;
    const size_t kvoff = ((size_t)(b * HKV + xg) * SK + (SK - WIN) + s * 256 + ((w + s + 3 * xg + 4 * b) & 7) * 32) * (DH * 2) + 4096;
    const GAS char* kp = (const GAS char*)kc + kvoff;
    const GAS char* vp = (const GAS char*)vc + kvoff;
    const u32 ko = n * 256 + g * 16, vo = g * 1024 + n * 16;
    u32x4 K[8], V[8];
    if (w >= 4) bar0();
#pragma unroll
    for (int u = 0; u < 8; ++u) K[u] = __builtin_nontemporal_load((const GAS u32x4*)(kp + ko + ((u >> 2) - 1) * 4096 + (u & 3) * 64));
    __builtin_amdgcn_sched_barrier(0);
    if (w < 4) bar0();
    bar0();
#pragma unroll
    for (int u = 0; u < 8; ++u) V[u] = __builtin_nontemporal_load((const GAS u32x4*)(vp + vo + ((u >> 2) - 1) * 4096 + (u & 3) * 256));
    __builtin_amdgcn_sched_barrier(0);
    const u32 tag = LOAD_TAG();
    STAMP(1);
    bar0();
    STAMP(2);
    f32x4 S0 = z4, S1 = z4;
#pragma unroll
    for (int ks = 0; ks < 4; ++ks) {
      const u32x4 qf = *(LAS u32x4*)(L + L_Q + (n & 3) * 272 + ks * 64 + g * 16);
      S0 = mfma(K[ks], qf, S0);
      S1 = mfma(K[4 + ks], qf, S1);
    }
    constexpr float C2 = 0.08838834764831845f * 1.4426950408889634f;
    float m = fmaxf(fmaxf(fmaxf(S0[0], S0[1]), fmaxf(S0[2], S0[3])), fmaxf(fmaxf(S1[0], S1[1]), fmaxf(S1[2], S1[3])));
    m = x16_32_max(m);
    if (lane < 4) *(LAS float*)(L + L_M + lane * 32 + w * 4) = m;
    bar();
    {
      const f32x4 ma = *(LAS f32x4*)(L + L_M + (n & 3) * 32), mb = *(LAS f32x4*)(L + L_M + (n & 3) * 32 + 16);
      m = fmaxf(fmaxf(fmaxf(ma[0], ma[1]), fmaxf(ma[2], ma[3])), fmaxf(fmaxf(mb[0], mb[1]), fmaxf(mb[2], mb[3]))) * C2;
    }
    u32x4 P;
    P.x = pk(__builtin_amdgcn_exp2f(fmaf(S0[0], C2, -m)), __builtin_amdgcn_exp2f(fmaf(S0[1], C2, -m)));
    P.y = pk(__builtin_amdgcn_exp2f(fmaf(S0[2], C2, -m)), __builtin_amdgcn_exp2f(fmaf(S0[3], C2, -m)));
    P.z = pk(__builtin_amdgcn_exp2f(fmaf(S1[0], C2, -m)), __builtin_amdgcn_exp2f(fmaf(S1[1], C2, -m)));
    P.w = pk(__builtin_amdgcn_exp2f(fmaf(S1[2], C2, -m)), __builtin_amdgcn_exp2f(fmaf(S1[3], C2, -m)));
    const f32x4 ls = mfma(u32x4{0x3f803f80u, 0x3f803f80u, 0x3f803f80u, 0x3f803f80u}, P, z4);
    if (lane < 4) *(LAS float*)(L + L_L + lane * 32 + w * 4) = ls[0];
    STAMP(3);
    f32x4 O[8];
#pragma unroll
    for (int d = 0; d < 8; ++d) {
      const u32 sel = (d & 1) ? 0x07060302u : 0x05040100u;
      u32x4 a;
#pragma unroll
      for (int e = 0; e < 4; ++e) a[e] = __builtin_amdgcn_perm(V[2 * e + 1][d >> 1], V[2 * e][d >> 1], sel);
      O[d] = mfma(a, P, z4);
    }
    if (n < 4) {
#pragma unroll
      for (int d = 0; d < 8; ++d) *(LAS f32x4*)(L + L_OW + w * OW_STRIDE + n * 528 + g * 128 + d * 16) = O[d];
    }
    STAMP(4);
    bar();
    {
      const int h = w >> 1;
      LAS char* src = L + L_OW + h * 528 + (w & 1) * 256 + lane * 4;
      float r[8];
#pragma unroll
      for (int u = 0; u < 8; ++u) r[u] = *(LAS float*)(src + u * OW_STRIDE);
      const f32x4 la = *(LAS f32x4*)(L + L_L + h * 32), lb = *(LAS f32x4*)(L + L_L + h * 32 + 16);
      const float v = ((r[0] + r[1]) + (r[2] + r[3])) + ((r[4] + r[5]) + (r[6] + r[7]));
      const float lsum = ((la[0] + la[1]) + (la[2] + la[3])) + ((lb[0] + lb[1]) + (lb[2] + lb[3]));
      const u32 p01 = pk(v, dpp<SHL4>(v)), p23 = dppu<SHL8>(p01);
      const u32 mh = __builtin_amdgcn_readlane(__float_as_uint(m), h);
      const bool dat = (lane & 12) == 0;
      const u32 slot = dat ? (lane >> 5) * 8 + (lane & 3) * 2 + (g & 1) : 16 + (lane & 3) + (g & 1) * 4;
      if (dat || ((lane & 44) == 4))
        put(ws, WS_OP + ((xg * 8 + w) * 32 + b * 16 + s) * 384 + slot * 16, dat ? p01 : mh, dat ? p23 : __float_as_uint(lsum), tag);
    }
    STAMP(5);
    bar0();
    const int r = xs * 8 + (i >> 2);
    {
      u32x4 v[1];
      gather<1>(ws, WS_OP + (r * 32 + w * 4 + g) * 384 + n * 16, 0, tag, v);
      STAMP(6);
      *(LAS u32x2*)(L + L_OS + (w * 4 + g) * 144 + ((n >> 3) * 32 + (n & 3) * 8 + ((n >> 2) & 1) * 4) * 2) = u32x2{v[0].x, v[0].z};
    }
    bar();
    STAMP(7);
    {
      const int q = (lane >> 2) & 3, p = lane & 3;
      LAS char* a = L + L_WO + (4 * g + q) * 256 + ((w ^ (4 * (g & 1) + q)) * 32) + p * 8;
      LAS char* os = L + L_OS + n * 144 + g * 16;
      const u32x4 w0 = trd2(a, 4096), w1 = trd2(a + 8192, 4096), w2 = trd2(a + 16384, 4096);
      f32x4 a0 = mfma(*(LAS u32x4*)os, w0, z4), a1 = mfma(*(LAS u32x4*)(os + 16 * 144), w0, z4);
      a0 = mfma(*(LAS u32x4*)(os + 64), w1, a0);
      a1 = mfma(*(LAS u32x4*)(os + 16 * 144 + 64), w1, a1);
      const f32x4 ff = mfma(*(LAS u32x4*)(L + L_H + (n & 1) * 64 + g * 16), w2, z4);
      const f32x4 s0 = *(LAS f32x4*)(L + L_W + g * 16), s1 = *(LAS f32x4*)(L + L_W + 64 + g * 16), gg = *(LAS f32x4*)(L + L_G);
      float r0 = x32_sum(x16_sum((a0[0] * s0[0] + a0[1] * s0[1]) + (a0[2] * s0[2] + a0[3] * s0[3]))) * gg[0];
      float r1 = x32_sum(x16_sum((a1[0] * s1[0] + a1[1] * s1[1]) + (a1[2] * s1[2] + a1[3] * s1[3]))) * gg[2];
      if ((i >> 3) * 8 + xs < 28) { r0 = fmaf(gg[1], ff[0], r0); r1 = fmaf(gg[3], ff[1], r1); }
      const float rr = (g & 1) ? r1 : r0, rn = dpp<XOR1>(rr);
      if ((lane & 33) == 0) put(ws, WS_FP + ((c * 8 + w) * 2 + g) * 8192 + r * 128 + n * 8, __float_as_uint(rr), __float_as_uint(rn), tag);
    }
    STAMP(8);
  } else {
    const u32 vrow128 = wave * 32 + (lane >> 3), vch128 = ((lane & 7) ^ (2 * (g & 1))) * 16;
    {
      const char* base = wq + (size_t)c * 128 * 8192 + (xs * 512 + (i >> 2) * 64) * 2;
      const u32 va = vrow128 * 8192 + vch128, vb = (va ^ 64) + 8 * 8192;
      dma2(base, va, vb, lbase + L_T0 + wave * 4096);
      dma2(base, va + 16 * 8192, vb + 16 * 8192, lbase + L_T0 + wave * 4096 + 2048);
    }
    u32x4 xf[4];
#pragma unroll
    for (int ks = 0; ks < 4; ++ks) xf[ks] = *(const GAS u32x4*)((const GAS char*)x + (n & 1) * 1024 + c * 256 + ks * 64 + g * 16);
    const u32 tag = LOAD_TAG();
    asm volatile("s_waitcnt vmcnt(0)" : "+v"(xf[0]), "+v"(xf[1]), "+v"(xf[2]), "+v"(xf[3]) :: "memory");
    STAMP(1);
    bar0();
    proj_put(L + L_T0, xf, wave, lane, ws, WS_QP + (xs * 8 + c) * 4096 + ((i >> 2) * 32 + wave * 8) * 16, 16384, tag);
    STAMP(2);
    if (wave == 3) {
      const u32x4 x0 = *(const GAS u32x4*)((const GAS char*)x + lane * 16), x1 = *(const GAS u32x4*)((const GAS char*)x + 1024 + lane * 16),
                  gv = *(const GAS u32x4*)((const GAS char*)gw + lane * 16);
      float z0 = 0.f, z1 = 0.f;
#pragma unroll
      for (int e = 0; e < 4; ++e) {
        z0 += rbf(blo(x0[e]) * blo(gv[e])) + rbf(bhi(x0[e]) * bhi(gv[e]));
        z1 += rbf(blo(x1[e]) * blo(gv[e])) + rbf(bhi(x1[e]) * bhi(gv[e]));
      }
      z0 = x32_sum(x16_sum(row_sum(z0))); z1 = x32_sum(x16_sum(row_sum(z1)));
      const float z = rbf((lane & 1) ? z1 : z0);
      const float sg = rbf(__builtin_amdgcn_rcpf(1.f + __expf(-z)));
      if (lane < 2) *(LAS u32x2*)(L + L_G + lane * 8) = u32x2{__float_as_uint(sg), __float_as_uint(rbf(1.f - sg))};
    }
    bar0();
    {
      u32x4 v[4];
      gather<4>(ws, WS_QP + (xg * 2 + b) * 16384 + (wave * 64 + lane) * 16, 4096, tag, v);
      const float q0 = (__uint_as_float(v[0].x) + __uint_as_float(v[1].x)) + (__uint_as_float(v[2].x) + __uint_as_float(v[3].x));
      const float q1 = (__uint_as_float(v[0].z) + __uint_as_float(v[1].z)) + (__uint_as_float(v[2].z) + __uint_as_float(v[3].z));
      *(LAS u32*)(L + L_Q + wave * 272 + lane * 4) = pk(q0, q1);
    }
    STAMP(3);
    bar();
    bar0();
    const int gp = (i >> 3) * 8 + xg, gc = (i >> 3) * 8 + xs;
    if (gp < 28) {
      const char* base = ((i & 4) ? wu : wg) + (size_t)c * 128 * (F * 2) + gp * 128;
      const u32 va = vrow128 * (F * 2) + vch128, vb = (va ^ 64) + 8 * F * 2;
      dma2(base, va, vb, lbase + L_T1 + wave * 4096);
      dma2(base, va + 16 * F * 2, vb + 16 * F * 2, lbase + L_T1 + wave * 4096 + 2048);
    }
    {
      const u32 va = g * 1024 + (n ^ (2 * g)) * 16, vb = (va ^ 128) + 4096;
      const char* base = wo + (size_t)(xs * 512 + (i >> 2) * 64 + wave * 16) * 1024 + c * 256;
      dma2(base, va, vb, lbase + L_WO + wave * 4096);
      dma2(base, va + 8192, vb + 8192, lbase + L_WO + wave * 4096 + 2048);
      const int row = gc < 28 ? gc * 64 + ((i >> 2) & 1) * 32 + wave * 8 : 0;
      dma2(wd + (size_t)row * 1024 + c * 256, va, vb, lbase + L_WO + 16384 + wave * 2048);
    }
    STAMP(4);
    bar0();
    asm volatile("s_waitcnt vmcnt(6)" ::: "memory");
    STAMP(5);
    bar0();
    if (gp < 28) proj_put(L + L_T1, xf, wave, lane, ws, WS_HP + (gp * 2 + (wave >> 1)) * 4096 + (i & 7) * 512 + (wave & 1) * 128, 256, tag);
    STAMP(6);
    if (wave == 1) {
      if (gc < 28) {
        u32x4 v[4];
        gather<4>(ws, WS_HP + (gc * 2 + ((i >> 2) & 1)) * 4096 + lane * 16, 1024, tag, v);
        const float g0 = rbf(x32_sum(__uint_as_float(v[0].x) + __uint_as_float(v[1].x))), g1 = rbf(x32_sum(__uint_as_float(v[0].z) + __uint_as_float(v[1].z)));
        const float u0 = rbf(x32_sum(__uint_as_float(v[2].x) + __uint_as_float(v[3].x))), u1 = rbf(x32_sum(__uint_as_float(v[2].z) + __uint_as_float(v[3].z)));
        const float h0 = rbf(g0 * __builtin_amdgcn_rcpf(1.f + __expf(-g0))) * u0, h1 = rbf(g1 * __builtin_amdgcn_rcpf(1.f + __expf(-g1))) * u1;
        if (lane < 32) *(LAS u32*)(L + L_H + g * 64 + (((n >> 1) & 3) * 8 + (n >> 3) * 4 + (n & 1) * 2) * 2) = pk(h0, h1);
      }
    } else if (wave == 2) {
      u32x4 v[1];
      gather<1>(ws, WS_OP + ((xs * 8 + (i >> 2)) * 32 + (lane & 31)) * 384 + 256, 0, tag, v, lane < 32);
      const float ms = __uint_as_float(v[0].x), M = row_max(ms), e = __builtin_amdgcn_exp2f(ms - M);
      const float Ls = row_sum(e * __uint_as_float(v[0].z));
      if (lane < 32) *(LAS float*)(L + L_W + lane * 4) = e / Ls;
    }
    asm volatile("s_waitcnt vmcnt(0)" ::: "memory");
    STAMP(7);
    bar();
    if (wave == 3 && xg < 2) {
      const int f = xg * 32 + i;
      u32x4 v[8];
      gather<8>(ws, WS_FP + f * 8192 + lane * 16, 1024, tag, v);
      STAMP(8);
      float r0 = 0.f, r1 = 0.f;
#pragma unroll
      for (int u = 0; u < 8; ++u) { r0 += __uint_as_float(v[u].x); r1 += __uint_as_float(v[u].z); }
      r0 += dpp<ROR8>(r0); r1 += dpp<ROR8>(r1);
      r0 = x32_sum(x16_sum(r0)); r1 = x32_sum(x16_sum(r1));
      if (lane < 8) ((GAS u32*)out)[(f & 1) * 256 + (f >> 1) * 8 + lane] = pk(r0, r1);
      if (f == 0 && lane == 0) __builtin_amdgcn_raw_buffer_store_b32(tag, ws, 0, 0, 16);
      STAMP(9);
    }
  }
  STAMP_FLUSH;
}

extern "C" void kernel_launch(void* const* d_in, const int* in_sizes, int n_in, void* d_out, int out_size, void* d_ws, size_t ws_size,
                              hipStream_t stream) {
  Args a{d_in[0], d_in[1], d_in[2], d_in[3], d_in[4], d_in[6], d_in[7], d_in[8], d_in[9], d_out, d_ws, d_ws};
  fused_decode<<<256, 768, 0, stream>>>(a);
}
```

```cpp
#include <hip/hip_runtime.h>
#include <hip/hip_bf16.h>

typedef unsigned short u16;
typedef unsigned u32;
typedef unsigned long long u64;
typedef u32 u32x4 __attribute__((ext_vector_type(4)));
typedef u32 u32x2 __attribute__((ext_vector_type(2)));
typedef u32 u32x8 __attribute__((ext_vector_type(8)));
typedef u32 u32x16 __attribute__((ext_vector_type(16)));
typedef float f32x4 __attribute__((ext_vector_type(4)));
typedef short s16x4 __attribute__((ext_vector_type(4)));
typedef __bf16 bf16x8 __attribute__((ext_vector_type(8)));
typedef __bf16 bf16x2 __attribute__((ext_vector_type(2)));
#define LAS __attribute__((address_space(3)))
#define GAS __attribute__((address_space(1)))
#define CAS __attribute__((address_space(4)))

#define STAMP_DECL
#define STAMP(i)
#define STAMP_FLUSH
#define XSH 4
#define PSLEEP 1

namespace {
constexpr int D = 512, HKV = 8, SK = 16384, DH = 128, WIN = 4096, F = 1792;
constexpr u32 WS_QP = 4096;
constexpr u32 WS_HP = WS_QP + 8 * 2 * 4 * 4096;
constexpr u32 WS_OP = WS_HP + 32 * 2 * 4096;
constexpr u32 WS_FP = WS_OP + 64 * 32 * 384;
constexpr u32 WS_END = WS_FP + 64 * 8192;
constexpr u32 L_T0 = 0, L_T1 = 16384, L_WO = 32768  , L_OW = L_WO + 24576  , OW_STRIDE = 4 * 528;
constexpr u32 L_Q = L_OW + 8 * OW_STRIDE  , L_M = L_Q + 4 * 272  , L_L = L_M + 128,
              L_OS = L_L + 128  , L_H = L_OS + 32 * 144  , L_W = L_H + 128  ,
              L_G = L_W + 128, L_END = L_G + 16;
constexpr int SPIN_CAP = 1 << 16;

__device__ __forceinline__ f32x4 mfma(u32x4 a, u32x4 b, f32x4 c) {
  return __builtin_amdgcn_mfma_f32_16x16x32_bf16(__builtin_bit_cast(bf16x8, a), __builtin_bit_cast(bf16x8, b), c, 0, 0, 0);
}
__device__ __forceinline__ u32 pk(float lo, float hi) {
  bf16x2 v = {(__bf16)lo, (__bf16)hi};
  return __builtin_bit_cast(u32, v);
}
__device__ __forceinline__ float rbf(float f) { return (float)(__bf16)f; }
__device__ __forceinline__ float blo(u32 v) { return __uint_as_float(v << 16); }
__device__ __forceinline__ float bhi(u32 v) { return __uint_as_float(v & 0xffff0000u); }
template <int CTRL> __device__ __forceinline__ float dpp(float x) {
  return __builtin_bit_cast(float, __builtin_amdgcn_mov_dpp(__builtin_bit_cast(int, x), CTRL, 0xf, 0xf, true));
}
template <int CTRL> __device__ __forceinline__ u32 dppu(u32 x) { return (u32)__builtin_amdgcn_mov_dpp((int)x, CTRL, 0xf, 0xf, true); }
constexpr int ROR1 = 0x121, ROR2 = 0x122, ROR4 = 0x124, ROR8 = 0x128, SHL4 = 0x104, SHL8 = 0x108, XOR1 = 0xB1;
__device__ __forceinline__ float row_sum(float x) { x += dpp<ROR8>(x); x += dpp<ROR4>(x); x += dpp<ROR2>(x); x += dpp<ROR1>(x); return x; }
__device__ __forceinline__ float row_max(float x) {
  x = fmaxf(x, dpp<ROR8>(x)); x = fmaxf(x, dpp<ROR4>(x)); x = fmaxf(x, dpp<ROR2>(x)); x = fmaxf(x, dpp<ROR1>(x)); return x;
}
__device__ __forceinline__ float x16_sum(float x) {
  auto s = __builtin_amdgcn_permlane16_swap(__float_as_uint(x), __float_as_uint(x), false, false);
  return __uint_as_float(s[0]) + __uint_as_float(s[1]);
}
__device__ __forceinline__ float x32_sum(float x) {
  auto s = __builtin_amdgcn_permlane32_swap(__float_as_uint(x), __float_as_uint(x), false, false);
  return __uint_as_float(s[0]) + __uint_as_float(s[1]);
}
__device__ __forceinline__ float x16_32_max(float x) {
  auto s = __builtin_amdgcn_permlane16_swap(__float_as_uint(x), __float_as_uint(x), false, false);
  x = fmaxf(__uint_as_float(s[0]), __uint_as_float(s[1]));
  auto t = __builtin_amdgcn_permlane32_swap(__float_as_uint(x), __float_as_uint(x), false, false);
  return fmaxf(__uint_as_float(t[0]), __uint_as_float(t[1]));
}
__device__ __forceinline__ void bar() { asm volatile("s_waitcnt lgkmcnt(0)\n\ts_barrier" ::: "memory"); }
__device__ __forceinline__ void bar0() { asm volatile("s_barrier" ::: "memory"); }
__device__ __forceinline__ void put(__amdgpu_buffer_rsrc_t ws, u32 off, u32 a, u32 b, u32 tag) {
  __builtin_amdgcn_raw_buffer_store_b128(u32x4{a, tag, b, tag}, ws, off, 0, 16);
}
__device__ __forceinline__ u32x4 get(__amdgpu_buffer_rsrc_t ws, u32 off) { return __builtin_amdgcn_raw_buffer_load_b128(ws, off, 0, 16); }
__device__ __forceinline__ bool fresh(u32x4 v, u32 tag) { return (v.y == tag) & (v.w == tag); }
template <int N> __device__ __forceinline__ void gather(__amdgpu_buffer_rsrc_t ws, u32 off, u32 stride, u32 tag, u32x4 (&v)[N], bool need = true) {
#pragma unroll
  for (int k = 0; k < N; ++k) v[k] = u32x4{0, 0, 0, 0};
#pragma unroll 1
  for (int spin = 0; spin < SPIN_CAP; ++spin) {
    asm volatile("" ::: "memory");
    if (need) {
      bool ok = true;
#pragma unroll
      for (int k = 0; k < N; ++k) v[k] = get(ws, off + k * stride);
#pragma unroll
      for (int k = 0; k < N; ++k) ok &= fresh(v[k], tag);
      need = !ok;
    }
    if (!__any(need)) break;
    __builtin_amdgcn_s_sleep(PSLEEP);
  }
}
__device__ __forceinline__ void dma2(const char* base, u32 va, u32 vb, u32 lds) {
  asm volatile("s_mov_b32 m0, %2\n\ts_nop 0\n\tglobal_load_lds_dwordx4 %0, %3 nt\n\ts_add_u32 m0, m0, 0x400\n\ts_nop 0\n\tglobal_load_lds_dwordx4 %1, %3 nt"
               :: "v"(va), "v"(vb), "s"(lds), "s"(base) : "memory", "scc");
}
__device__ __forceinline__ u32x2 trd(LAS char* p) {
  return __builtin_bit_cast(u32x2, __builtin_amdgcn_ds_read_tr16_b64_v4i16((LAS s16x4*)p));
}
__device__ __forceinline__ u32x4 trd2(LAS char* p, int d) { const u32x2 lo = trd(p), hi = trd(p + d); return u32x4{lo.x, lo.y, hi.x, hi.y}; }
__device__ __forceinline__ void proj_put(LAS char* tile, const u32x4 (&xf)[4], int t, int lane, __amdgpu_buffer_rsrc_t ws, u32 off, u32 bstride, u32 tag) {
  const int g = lane >> 4, q = (lane >> 2) & 3, p = lane & 3, f = (q >> 1) | ((g & 1) << 1);
  LAS char* a = tile + (8 * g + q) * 128 + ((t ^ f) * 32) + p * 8;
  const f32x4 z = {0.f, 0.f, 0.f, 0.f};
  f32x4 c0 = mfma(trd2(a, 512), xf[0], z), c1 = mfma(trd2(a + 4096, 512), xf[1], z);
  c0 = mfma(trd2(a + 8192, 512), xf[2], c0);
  c1 = mfma(trd2(a + 12288, 512), xf[3], c1);
  c0 += c1;
  const bool up = lane & 2;
  if ((lane & 12) == 0)
    put(ws, off + (lane & 1) * bstride + (g * 2 + ((lane >> 1) & 1)) * 16, __float_as_uint(up ? c0[2] : c0[0]), __float_as_uint(up ? c0[3] : c0[1]), tag);
}
}

struct Args { const void *x, *kc, *vc, *wq, *wo, *wg, *wu, *wd, *gw; void *out, *ws, *ws2; };

__global__ void __launch_bounds__(768, 3) fused_decode(Args) {
  __shared__ __attribute__((aligned(1024))) char smem[L_END];
  LAS char* L = (LAS char*)smem;
  const u32 lbase = (u32)(size_t)L;
  STAMP_DECL;
  STAMP(0);
  u32x16 ka; u32x8 kb;
  asm volatile("s_load_dwordx16 %0, %2, 0x0\n\ts_load_dwordx8 %1, %2, 0x40\n\ts_waitcnt lgkmcnt(0)"
               : "=&s"(ka), "=&s"(kb) : "s"(__builtin_amdgcn_kernarg_segment_ptr()));
  STAMP(10);
#define PA(i) ((const char*)(((u64)ka[2 * (i) + 1] << 32) | ka[2 * (i)]))
#define PB(i) ((char*)(((u64)kb[2 * (i) + 1] << 32) | kb[2 * (i)]))
  const char *x = PA(0), *kc = PA(1), *vc = PA(2), *wq = PA(3), *wo = PA(4), *wg = PA(5), *wu = PA(6), *wd = PA(7), *gw = PB(0);
  char *out = PB(1), *wsp = PB(2);
  const int j = blockIdx.x, xg = j & 7, i = j >> 3, xs = (xg + XSH) & 7;
  const int wave = __builtin_amdgcn_readfirstlane(threadIdx.x >> 6), lane = threadIdx.x & 63;
  const int g = lane >> 4, n = lane & 15;
  const int b = i & 1, s = i >> 1, c = i & 3;
  const __amdgpu_buffer_rsrc_t ws = __builtin_amdgcn_make_buffer_rsrc(wsp, 0, WS_END, 0x00020000);
  const f32x4 z4 = {0.f, 0.f, 0.f, 0.f};
#define LOAD_TAG() (*(const CAS u32*)wsp * 1664525u + 1013904223u)

  if (wave >= 4) {
    const int w = wave - 4;
    const size_t kvoff = ((size_t)(b * HKV + xg) * SK + (SK - WIN) + s * 256 + ((w + s + 3 * xg + 4 * b) & 7) * 32) * (DH * 2) + 4096;
    const GAS char* kp = (const GAS char*)kc + kvoff;
    const GAS char* vp = (const GAS char*)vc + kvoff;
    const u32 ko = n * 256 + g * 16, vo = g * 1024 + n * 16;
    u32x4 K[8], V[8];
    if (w >= 4) bar0();
#pragma unroll
    for (int u = 0; u < 8; ++u) K[u] = __builtin_nontemporal_load((const GAS u32x4*)(kp + ko + ((u >> 2) - 1) * 4096 + (u & 3) * 64));
    __builtin_amdgcn_sched_barrier(0);
    if (w < 4) bar0();
    bar0();
#pragma unroll
    for (int u = 0; u < 8; ++u) V[u] = __builtin_nontemporal_load((const GAS u32x4*)(vp + vo + ((u >> 2) - 1) * 4096 + (u & 3) * 256));
    __builtin_amdgcn_sched_barrier(0);
    const u32 tag = LOAD_TAG();
    STAMP(1);
    bar0();
    STAMP(2);
    f32x4 S0 = z4, S1 = z4;
#pragma unroll
    for (int ks = 0; ks < 4; ++ks) {
      const u32x4 qf = *(LAS u32x4*)(L + L_Q + (n & 3) * 272 + ks * 64 + g * 16);
      S0 = mfma(K[ks], qf, S0);
      S1 = mfma(K[4 + ks], qf, S1);
    }
    constexpr float C2 = 0.08838834764831845f * 1.4426950408889634f;
    float m = fmaxf(fmaxf(fmaxf(S0[0], S0[1]), fmaxf(S0[2], S0[3])), fmaxf(fmaxf(S1[0], S1[1]), fmaxf(S1[2], S1[3])));
    m = x16_32_max(m);
    if (lane < 4) *(LAS float*)(L + L_M + lane * 32 + w * 4) = m;
    bar();
    {
      const f32x4 ma = *(LAS f32x4*)(L + L_M + (n & 3) * 32), mb = *(LAS f32x4*)(L + L_M + (n & 3) * 32 + 16);
      m = fmaxf(fmaxf(fmaxf(ma[0], ma[1]), fmaxf(ma[2], ma[3])), fmaxf(fmaxf(mb[0], mb[1]), fmaxf(mb[2], mb[3]))) * C2;
    }
    u32x4 P;
    P.x = pk(__builtin_amdgcn_exp2f(fmaf(S0[0], C2, -m)), __builtin_amdgcn_exp2f(fmaf(S0[1], C2, -m)));
    P.y = pk(__builtin_amdgcn_exp2f(fmaf(S0[2], C2, -m)), __builtin_amdgcn_exp2f(fmaf(S0[3], C2, -m)));
    P.z = pk(__builtin_amdgcn_exp2f(fmaf(S1[0], C2, -m)), __builtin_amdgcn_exp2f(fmaf(S1[1], C2, -m)));
    P.w = pk(__builtin_amdgcn_exp2f(fmaf(S1[2], C2, -m)), __builtin_amdgcn_exp2f(fmaf(S1[3], C2, -m)));
    const f32x4 ls = mfma(u32x4{0x3f803f80u, 0x3f803f80u, 0x3f803f80u, 0x3f803f80u}, P, z4);
    if (lane < 4) *(LAS float*)(L + L_L + lane * 32 + w * 4) = ls[0];
    STAMP(3);
    f32x4 O[8];
#pragma unroll
    for (int d = 0; d < 8; ++d) {
      const u32 sel = (d & 1) ? 0x07060302u : 0x05040100u;
      u32x4 a;
#pragma unroll
      for (int e = 0; e < 4; ++e) a[e] = __builtin_amdgcn_perm(V[2 * e + 1][d >> 1], V[2 * e][d >> 1], sel);
      O[d] = mfma(a, P, z4);
    }
    if (n < 4) {
#pragma unroll
      for (int d = 0; d < 8; ++d) *(LAS f32x4*)(L + L_OW + w * OW_STRIDE + n * 528 + g * 128 + d * 16) = O[d];
    }
    STAMP(4);
    bar();
    {
      const int h = w >> 1;
      LAS char* src = L + L_OW + h * 528 + (w & 1) * 256 + lane * 4;
      float r[8];
#pragma unroll
      for (int u = 0; u < 8; ++u) r[u] = *(LAS float*)(src + u * OW_STRIDE);
      const f32x4 la = *(LAS f32x4*)(L + L_L + h * 32), lb = *(LAS f32x4*)(L + L_L + h * 32 + 16);
      const float v = ((r[0] + r[1]) + (r[2] + r[3])) + ((r[4] + r[5]) + (r[6] + r[7]));
      const float lsum = ((la[0] + la[1]) + (la[2] + la[3])) + ((lb[0] + lb[1]) + (lb[2] + lb[3]));
      const u32 p01 = pk(v, dpp<SHL4>(v)), p23 = dppu<SHL8>(p01);
      const u32 mh = __builtin_amdgcn_readlane(__float_as_uint(m), h);
      const bool dat = (lane & 12) == 0;
      const u32 slot = dat ? (lane >> 5) * 8 + (lane & 3) * 2 + (g & 1) : 16 + (lane & 3) + (g & 1) * 4;
      if (dat || ((lane & 44) == 4))
        put(ws, WS_OP + ((xg * 8 + w) * 32 + b * 16 + s) * 384 + slot * 16, dat ? p01 : mh, dat ? p23 : __float_as_uint(lsum), tag);
    }
    STAMP(5);
    bar0();
    const int r = xs * 8 + (i >> 2);
    {
      u32x4 v[1];
      gather<1>(ws, WS_OP + (r * 32 + w * 4 + g) * 384 + n * 16, 0, tag, v);
      STAMP(6);
      *(LAS u32x2*)(L + L_OS + (w * 4 + g) * 144 + ((n >> 3) * 32 + (n & 3) * 8 + ((n >> 2) & 1) * 4) * 2) = u32x2{v[0].x, v[0].z};
    }
    bar();
    STAMP(7);
    {
      const int q = (lane >> 2) & 3, p = lane & 3;
      LAS char* a = L + L_WO + (4 * g + q) * 256 + ((w ^ (4 * (g & 1) + q)) * 32) + p * 8;
      LAS char* os = L + L_OS + n * 144 + g * 16;
      const u32x4 w0 = trd2(a, 4096), w1 = trd2(a + 8192, 4096), w2 = trd2(a + 16384, 4096);
      f32x4 a0 = mfma(*(LAS u32x4*)os, w0, z4), a1 = mfma(*(LAS u32x4*)(os + 16 * 144), w0, z4);
      a0 = mfma(*(LAS u32x4*)(os + 64), w1, a0);
      a1 = mfma(*(LAS u32x4*)(os + 16 * 144 + 64), w1, a1);
      const f32x4 ff = mfma(*(LAS u32x4*)(L + L_H + (n & 1) * 64 + g * 16), w2, z4);
      const f32x4 s0 = *(LAS f32x4*)(L + L_W + g * 16), s1 = *(LAS f32x4*)(L + L_W + 64 + g * 16), gg = *(LAS f32x4*)(L + L_G);
      float r0 = x32_sum(x16_sum((a0[0] * s0[0] + a0[1] * s0[1]) + (a0[2] * s0[2] + a0[3] * s0[3]))) * gg[0];
      float r1 = x32_sum(x16_sum((a1[0] * s1[0] + a1[1] * s1[1]) + (a1[2] * s1[2] + a1[3] * s1[3]))) * gg[2];
      if ((i >> 3) * 8 + xs < 28) { r0 = fmaf(gg[1], ff[0], r0); r1 = fmaf(gg[3], ff[1], r1); }
      const float rr = (g & 1) ? r1 : r0, rn = dpp<XOR1>(rr);
      if ((lane & 33) == 0) put(ws, WS_FP + ((c * 8 + w) * 2 + g) * 8192 + r * 128 + n * 8, __float_as_uint(rr), __float_as_uint(rn), tag);
    }
    STAMP(8);
  } else {
    const u32 vrow128 = wave * 32 + (lane >> 3), vch128 = ((lane & 7) ^ (2 * (g & 1))) * 16;
    {
      const char* base = wq + (size_t)c * 128 * 8192 + (xs * 512 + (i >> 2) * 64) * 2;
      const u32 va = vrow128 * 8192 + vch128, vb = (va ^ 64) + 8 * 8192;
      dma2(base, va, vb, lbase + L_T0 + wave * 4096);
      dma2(base, va + 16 * 8192, vb + 16 * 8192, lbase + L_T0 + wave * 4096 + 2048);
    }
    u32x4 xf[4];
#pragma unroll
    for (int ks = 0; ks < 4; ++ks) xf[ks] = *(const GAS u32x4*)((const GAS char*)x + (n & 1) * 1024 + c * 256 + ks * 64 + g * 16);
    const u32 tag = LOAD_TAG();
    asm volatile("s_waitcnt vmcnt(0)" : "+v"(xf[0]), "+v"(xf[1]), "+v"(xf[2]), "+v"(xf[3]) :: "memory");
    STAMP(1);
    bar0();
    proj_put(L + L_T0, xf, wave, lane, ws, WS_QP + (xs * 8 + c) * 4096 + ((i >> 2) * 32 + wave * 8) * 16, 16384, tag);
    STAMP(2);
    if (wave == 3) {
      const u32x4 x0 = *(const GAS u32x4*)((const GAS char*)x + lane * 16), x1 = *(const GAS u32x4*)((const GAS char*)x + 1024 + lane * 16),
                  gv = *(const GAS u32x4*)((const GAS char*)gw + lane * 16);
      float z0 = 0.f, z1 = 0.f;
#pragma unroll
      for (int e = 0; e < 4; ++e) {
        z0 += rbf(blo(x0[e]) * blo(gv[e])) + rbf(bhi(x0[e]) * bhi(gv[e]));
        z1 += rbf(blo(x1[e]) * blo(gv[e])) + rbf(bhi(x1[e]) * bhi(gv[e]));
      }
      z0 = x32_sum(x16_sum(row_sum(z0))); z1 = x32_sum(x16_sum(row_sum(z1)));
      const float z = rbf((lane & 1) ? z1 : z0);
      const float sg = rbf(__builtin_amdgcn_rcpf(1.f + __expf(-z)));
      if (lane < 2) *(LAS u32x2*)(L + L_G + lane * 8) = u32x2{__float_as_uint(sg), __float_as_uint(rbf(1.f - sg))};
    }
    bar0();
    {
      u32x4 v[4];
      gather<4>(ws, WS_QP + (xg * 2 + b) * 16384 + (wave * 64 + lane) * 16, 4096, tag, v);
      const float q0 = (__uint_as_float(v[0].x) + __uint_as_float(v[1].x)) + (__uint_as_float(v[2].x) + __uint_as_float(v[3].x));
      const float q1 = (__uint_as_float(v[0].z) + __uint_as_float(v[1].z)) + (__uint_as_float(v[2].z) + __uint_as_float(v[3].z));
      *(LAS u32*)(L + L_Q + wave * 272 + lane * 4) = pk(q0, q1);
    }
    STAMP(3);
    bar();
    bar0();
    const int gp = (i >> 3) * 8 + xg, gc = (i >> 3) * 8 + xs;
    if (gp < 28) {
      const char* base = ((i & 4) ? wu : wg) + (size_t)c * 128 * (F * 2) + gp * 128;
      const u32 va = vrow128 * (F * 2) + vch128, vb = (va ^ 64) + 8 * F * 2;
      dma2(base, va, vb, lbase + L_T1 + wave * 4096);
      dma2(base, va + 16 * F * 2, vb + 16 * F * 2, lbase + L_T1 + wave * 4096 + 2048);
    }
    {
      const u32 va = g * 1024 + (n ^ (2 * g)) * 16, vb = (va ^ 128) + 4096;
      const char* base = wo + (size_t)(xs * 512 + (i >> 2) * 64 + wave * 16) * 1024 + c * 256;
      dma2(base, va, vb, lbase + L_WO + wave * 4096);
      dma2(base, va + 8192, vb + 8192, lbase + L_WO + wave * 4096 + 2048);
      const int row = gc < 28 ? gc * 64 + ((i >> 2) & 1) * 32 + wave * 8 : 0;
      dma2(wd + (size_t)row * 1024 + c * 256, va, vb, lbase + L_WO + 16384 + wave * 2048);
    }
    STAMP(4);
    bar0();
    asm volatile("s_waitcnt vmcnt(6)" ::: "memory");
    STAMP(5);
    bar0();
    if (gp < 28) proj_put(L + L_T1, xf, wave, lane, ws, WS_HP + (gp * 2 + (wave >> 1)) * 4096 + (i & 7) * 512 + (wave & 1) * 128, 256, tag);
    STAMP(6);
    if (wave == 1) {
      if (gc < 28) {
        u32x4 v[4];
        gather<4>(ws, WS_HP + (gc * 2 + ((i >> 2) & 1)) * 4096 + lane * 16, 1024, tag, v);
        const float g0 = rbf(x32_sum(__uint_as_float(v[0].x) + __uint_as_float(v[1].x))), g1 = rbf(x32_sum(__uint_as_float(v[0].z) + __uint_as_float(v[1].z)));
        const float u0 = rbf(x32_sum(__uint_as_float(v[2].x) + __uint_as_float(v[3].x))), u1 = rbf(x32_sum(__uint_as_float(v[2].z) + __uint_as_float(v[3].z)));
        const float h0 = rbf(g0 * __builtin_amdgcn_rcpf(1.f + __expf(-g0))) * u0, h1 = rbf(g1 * __builtin_amdgcn_rcpf(1.f + __expf(-g1))) * u1;
        if (lane < 32) *(LAS u32*)(L + L_H + g * 64 + (((n >> 1) & 3) * 8 + (n >> 3) * 4 + (n & 1) * 2) * 2) = pk(h0, h1);
      }
    } else if (wave == 2) {
      u32x4 v[1];
      gather<1>(ws, WS_OP + ((xs * 8 + (i >> 2)) * 32 + (lane & 31)) * 384 + 256, 0, tag, v, lane < 32);
      const float ms = __uint_as_float(v[0].x), M = row_max(ms), e = __builtin_amdgcn_exp2f(ms - M);
      const float Ls = row_sum(e * __uint_as_float(v[0].z));
      if (lane < 32) *(LAS float*)(L + L_W + lane * 4) = e / Ls;
    }
    asm volatile("s_waitcnt vmcnt(0)" ::: "memory");
    STAMP(7);
    bar();
    if (wave == 3 && xg < 2) {
      const int f = xg * 32 + i;
      u32x4 v[8];
      gather<8>(ws, WS_FP + f * 8192 + lane * 16, 1024, tag, v);
      STAMP(8);
      float r0 = 0.f, r1 = 0.f;
#pragma unroll
      for (int u = 0; u < 8; ++u) { r0 += __uint_as_float(v[u].x); r1 += __uint_as_float(v[u].z); }
      r0 += dpp<ROR8>(r0); r1 += dpp<ROR8>(r1);
      r0 = x32_sum(x16_sum(r0)); r1 = x32_sum(x16_sum(r1));
      if (lane < 8) ((GAS u32*)out)[(f & 1) * 256 + (f >> 1) * 8 + lane] = pk(r0, r1);
      if (f == 0 && lane == 0) __builtin_amdgcn_raw_buffer_store_b32(tag, ws, 0, 0, 16);
      STAMP(9);
    }
  }
  STAMP_FLUSH;
}

__global__ void tiny(u64* p){ }
extern "C" void kernel_launch(void* const* d_in, const int* in_sizes, int n_in, void* d_out, int out_size, void* d_ws, size_t ws_size,
                              hipStream_t stream) {
  Args a{d_in[0], d_in[1], d_in[2], d_in[3], d_in[4], d_in[6], d_in[7], d_in[8], d_in[9], d_out, d_ws, d_ws};
  tiny<<<1,64,0,stream>>>((u64*)((char*)d_ws+1024));
  fused_decode<<<256, 768, 0, stream>>>(a);
}
```

```cpp
#define STAMP_DECL unsigned long long ts_[12]={0,0,0,0,0,0,0,0,0,0,0,0}
#define STAMP(i) asm volatile("s_memrealtime %0\n\ts_waitcnt lgkmcnt(0)":"=s"(ts_[i])::"memory")
#define STAMP_FLUSH do{ if(__builtin_amdgcn_readfirstlane(threadIdx.x>>6)==0 && (blockIdx.x==0)){ unsigned long long T_=*(const CAS u64*)(wsp+1024)+4000; while(__builtin_amdgcn_s_memrealtime()<T_) __builtin_amdgcn_s_sleep(1);} }while(0)
#include <hip/hip_runtime.h>
#include <hip/hip_bf16.h>

typedef unsigned short u16;
typedef unsigned u32;
typedef unsigned long long u64;
typedef u32 u32x4 __attribute__((ext_vector_type(4)));
typedef u32 u32x2 __attribute__((ext_vector_type(2)));
typedef u32 u32x8 __attribute__((ext_vector_type(8)));
typedef u32 u32x16 __attribute__((ext_vector_type(16)));
typedef float f32x4 __attribute__((ext_vector_type(4)));
typedef short s16x4 __attribute__((ext_vector_type(4)));
typedef __bf16 bf16x8 __attribute__((ext_vector_type(8)));
typedef __bf16 bf16x2 __attribute__((ext_vector_type(2)));
#define LAS __attribute__((address_space(3)))
#define GAS __attribute__((address_space(1)))
#define CAS __attribute__((address_space(4)))

#define XSH 4
#define PSLEEP 1

namespace {
constexpr int D = 512, HKV = 8, SK = 16384, DH = 128, WIN = 4096, F = 1792;
constexpr u32 WS_QP = 4096;
constexpr u32 WS_HP = WS_QP + 8 * 2 * 4 * 4096;
constexpr u32 WS_OP = WS_HP + 32 * 2 * 4096;
constexpr u32 WS_FP = WS_OP + 64 * 32 * 384;
constexpr u32 WS_END = WS_FP + 64 * 8192;
constexpr u32 L_T0 = 0, L_T1 = 16384, L_WO = 32768  , L_OW = L_WO + 24576  , OW_STRIDE = 4 * 528;
constexpr u32 L_Q = L_OW + 8 * OW_STRIDE  , L_M = L_Q + 4 * 272  , L_L = L_M + 128,
              L_OS = L_L + 128  , L_H = L_OS + 32 * 144  , L_W = L_H + 128  ,
              L_G = L_W + 128, L_END = L_G + 16;
constexpr int SPIN_CAP = 1 << 16;

__device__ __forceinline__ f32x4 mfma(u32x4 a, u32x4 b, f32x4 c) {
  return __builtin_amdgcn_mfma_f32_16x16x32_bf16(__builtin_bit_cast(bf16x8, a), __builtin_bit_cast(bf16x8, b), c, 0, 0, 0);
}
__device__ __forceinline__ u32 pk(float lo, float hi) {
  bf16x2 v = {(__bf16)lo, (__bf16)hi};
  return __builtin_bit_cast(u32, v);
}
__device__ __forceinline__ float rbf(float f) { return (float)(__bf16)f; }
__device__ __forceinline__ float blo(u32 v) { return __uint_as_float(v << 16); }
__device__ __forceinline__ float bhi(u32 v) { return __uint_as_float(v & 0xffff0000u); }
template <int CTRL> __device__ __forceinline__ float dpp(float x) {
  return __builtin_bit_cast(float, __builtin_amdgcn_mov_dpp(__builtin_bit_cast(int, x), CTRL, 0xf, 0xf, true));
}
template <int CTRL> __device__ __forceinline__ u32 dppu(u32 x) { return (u32)__builtin_amdgcn_mov_dpp((int)x, CTRL, 0xf, 0xf, true); }
constexpr int ROR1 = 0x121, ROR2 = 0x122, ROR4 = 0x124, ROR8 = 0x128, SHL4 = 0x104, SHL8 = 0x108, XOR1 = 0xB1;
__device__ __forceinline__ float row_sum(float x) { x += dpp<ROR8>(x); x += dpp<ROR4>(x); x += dpp<ROR2>(x); x += dpp<ROR1>(x); return x; }
__device__ __forceinline__ float row_max(float x) {
  x = fmaxf(x, dpp<ROR8>(x)); x = fmaxf(x, dpp<ROR4>(x)); x = fmaxf(x, dpp<ROR2>(x)); x = fmaxf(x, dpp<ROR1>(x)); return x;
}
__device__ __forceinline__ float x16_sum(float x) {
  auto s = __builtin_amdgcn_permlane16_swap(__float_as_uint(x), __float_as_uint(x), false, false);
  return __uint_as_float(s[0]) + __uint_as_float(s[1]);
}
__device__ __forceinline__ float x32_sum(float x) {
  auto s = __builtin_amdgcn_permlane32_swap(__float_as_uint(x), __float_as_uint(x), false, false);
  return __uint_as_float(s[0]) + __uint_as_float(s[1]);
}
__device__ __forceinline__ float x16_32_max(float x) {
  auto s = __builtin_amdgcn_permlane16_swap(__float_as_uint(x), __float_as_uint(x), false, false);
  x = fmaxf(__uint_as_float(s[0]), __uint_as_float(s[1]));
  auto t = __builtin_amdgcn_permlane32_swap(__float_as_uint(x), __float_as_uint(x), false, false);
  return fmaxf(__uint_as_float(t[0]), __uint_as_float(t[1]));
}
__device__ __forceinline__ void bar() { asm volatile("s_waitcnt lgkmcnt(0)\n\ts_barrier" ::: "memory"); }
__device__ __forceinline__ void bar0() { asm volatile("s_barrier" ::: "memory"); }
__device__ __forceinline__ void put(__amdgpu_buffer_rsrc_t ws, u32 off, u32 a, u32 b, u32 tag) {
  __builtin_amdgcn_raw_buffer_store_b128(u32x4{a, tag, b, tag}, ws, off, 0, 16);
}
__device__ __forceinline__ u32x4 get(__amdgpu_buffer_rsrc_t ws, u32 off) { return __builtin_amdgcn_raw_buffer_load_b128(ws, off, 0, 16); }
__device__ __forceinline__ bool fresh(u32x4 v, u32 tag) { return (v.y == tag) & (v.w == tag); }
template <int N> __device__ __forceinline__ void gather(__amdgpu_buffer_rsrc_t ws, u32 off, u32 stride, u32 tag, u32x4 (&v)[N], bool need = true) {
#pragma unroll
  for (int k = 0; k < N; ++k) v[k] = u32x4{0, 0, 0, 0};
#pragma unroll 1
  for (int spin = 0; spin < SPIN_CAP; ++spin) {
    asm volatile("" ::: "memory");
    if (need) {
      bool ok = true;
#pragma unroll
      for (int k = 0; k < N; ++k) v[k] = get(ws, off + k * stride);
#pragma unroll
      for (int k = 0; k < N; ++k) ok &= fresh(v[k], tag);
      need = !ok;
    }
    if (!__any(need)) break;
    __builtin_amdgcn_s_sleep(PSLEEP);
  }
}
__device__ __forceinline__ void dma2(const char* base, u32 va, u32 vb, u32 lds) {
  asm volatile("s_mov_b32 m0, %2\n\ts_nop 0\n\tglobal_load_lds_dwordx4 %0, %3 nt\n\ts_add_u32 m0, m0, 0x400\n\ts_nop 0\n\tglobal_load_lds_dwordx4 %1, %3 nt"
               :: "v"(va), "v"(vb), "s"(lds), "s"(base) : "memory", "scc");
}
__device__ __forceinline__ u32x2 trd(LAS char* p) {
  return __builtin_bit_cast(u32x2, __builtin_amdgcn_ds_read_tr16_b64_v4i16((LAS s16x4*)p));
}
__device__ __forceinline__ u32x4 trd2(LAS char* p, int d) { const u32x2 lo = trd(p), hi = trd(p + d); return u32x4{lo.x, lo.y, hi.x, hi.y}; }
__device__ __forceinline__ void proj_put(LAS char* tile, const u32x4 (&xf)[4], int t, int lane, __amdgpu_buffer_rsrc_t ws, u32 off, u32 bstride, u32 tag) {
  const int g = lane >> 4, q = (lane >> 2) & 3, p = lane & 3, f = (q >> 1) | ((g & 1) << 1);
  LAS char* a = tile + (8 * g + q) * 128 + ((t ^ f) * 32) + p * 8;
  const f32x4 z = {0.f, 0.f, 0.f, 0.f};
  f32x4 c0 = mfma(trd2(a, 512), xf[0], z), c1 = mfma(trd2(a + 4096, 512), xf[1], z);
  c0 = mfma(trd2(a + 8192, 512), xf[2], c0);
  c1 = mfma(trd2(a + 12288, 512), xf[3], c1);
  c0 += c1;
  const bool up = lane & 2;
  if ((lane & 12) == 0)
    put(ws, off + (lane & 1) * bstride + (g * 2 + ((lane >> 1) & 1)) * 16, __float_as_uint(up ? c0[2] : c0[0]), __float_as_uint(up ? c0[3] : c0[1]), tag);
}
}

struct Args { const void *x, *kc, *vc, *wq, *wo, *wg, *wu, *wd, *gw; void *out, *ws, *ws2; };

__global__ void __launch_bounds__(768, 3) fused_decode(Args) {
  __shared__ __attribute__((aligned(1024))) char smem[L_END];
  LAS char* L = (LAS char*)smem;
  const u32 lbase = (u32)(size_t)L;
  STAMP_DECL;
  STAMP(0);
  u32x16 ka; u32x8 kb;
  asm volatile("s_load_dwordx16 %0, %2, 0x0\n\ts_load_dwordx8 %1, %2, 0x40\n\ts_waitcnt lgkmcnt(0)"
               : "=&s"(ka), "=&s"(kb) : "s"(__builtin_amdgcn_kernarg_segment_ptr()));
  STAMP(10);
#define PA(i) ((const char*)(((u64)ka[2 * (i) + 1] << 32) | ka[2 * (i)]))
#define PB(i) ((char*)(((u64)kb[2 * (i) + 1] << 32) | kb[2 * (i)]))
  const char *x = PA(0), *kc = PA(1), *vc = PA(2), *wq = PA(3), *wo = PA(4), *wg = PA(5), *wu = PA(6), *wd = PA(7), *gw = PB(0);
  char *out = PB(1), *wsp = PB(2);
  const int j = blockIdx.x, xg = j & 7, i = j >> 3, xs = (xg + XSH) & 7;
  const int wave = __builtin_amdgcn_readfirstlane(threadIdx.x >> 6), lane = threadIdx.x & 63;
  const int g = lane >> 4, n = lane & 15;
  const int b = i & 1, s = i >> 1, c = i & 3;
  const __amdgpu_buffer_rsrc_t ws = __builtin_amdgcn_make_buffer_rsrc(wsp, 0, WS_END, 0x00020000);
  const f32x4 z4 = {0.f, 0.f, 0.f, 0.f};
#define LOAD_TAG() (*(const CAS u32*)wsp * 1664525u + 1013904223u)

  if (wave >= 4) {
    const int w = wave - 4;
    const size_t kvoff = ((size_t)(b * HKV + xg) * SK + (SK - WIN) + s * 256 + ((w + s + 3 * xg + 4 * b) & 7) * 32) * (DH * 2) + 4096;
    const GAS char* kp = (const GAS char*)kc + kvoff;
    const GAS char* vp = (const GAS char*)vc + kvoff;
    const u32 ko = n * 256 + g * 16, vo = g * 1024 + n * 16;
    u32x4 K[8], V[8];
    if (w >= 4) bar0();
#pragma unroll
    for (int u = 0; u < 8; ++u) K[u] = __builtin_nontemporal_load((const GAS u32x4*)(kp + ko + ((u >> 2) - 1) * 4096 + (u & 3) * 64));
    __builtin_amdgcn_sched_barrier(0);
    if (w < 4) bar0();
    bar0();
#pragma unroll
    for (int u = 0; u < 8; ++u) V[u] = __builtin_nontemporal_load((const GAS u32x4*)(vp + vo + ((u >> 2) - 1) * 4096 + (u & 3) * 256));
    __builtin_amdgcn_sched_barrier(0);
    const u32 tag = LOAD_TAG();
    STAMP(1);
    bar0();
    STAMP(2);
    f32x4 S0 = z4, S1 = z4;
#pragma unroll
    for (int ks = 0; ks < 4; ++ks) {
      const u32x4 qf = *(LAS u32x4*)(L + L_Q + (n & 3) * 272 + ks * 64 + g * 16);
      S0 = mfma(K[ks], qf, S0);
      S1 = mfma(K[4 + ks], qf, S1);
    }
    constexpr float C2 = 0.08838834764831845f * 1.4426950408889634f;
    float m = fmaxf(fmaxf(fmaxf(S0[0], S0[1]), fmaxf(S0[2], S0[3])), fmaxf(fmaxf(S1[0], S1[1]), fmaxf(S1[2], S1[3])));
    m = x16_32_max(m);
    if (lane < 4) *(LAS float*)(L + L_M + lane * 32 + w * 4) = m;
    bar();
    {
      const f32x4 ma = *(LAS f32x4*)(L + L_M + (n & 3) * 32), mb = *(LAS f32x4*)(L + L_M + (n & 3) * 32 + 16);
      m = fmaxf(fmaxf(fmaxf(ma[0], ma[1]), fmaxf(ma[2], ma[3])), fmaxf(fmaxf(mb[0], mb[1]), fmaxf(mb[2], mb[3]))) * C2;
    }
    u32x4 P;
    P.x = pk(__builtin_amdgcn_exp2f(fmaf(S0[0], C2, -m)), __builtin_amdgcn_exp2f(fmaf(S0[1], C2, -m)));
    P.y = pk(__builtin_amdgcn_exp2f(fmaf(S0[2], C2, -m)), __builtin_amdgcn_exp2f(fmaf(S0[3], C2, -m)));
    P.z = pk(__builtin_amdgcn_exp2f(fmaf(S1[0], C2, -m)), __builtin_amdgcn_exp2f(fmaf(S1[1], C2, -m)));
    P.w = pk(__builtin_amdgcn_exp2f(fmaf(S1[2], C2, -m)), __builtin_amdgcn_exp2f(fmaf(S1[3], C2, -m)));
    const f32x4 ls = mfma(u32x4{0x3f803f80u, 0x3f803f80u, 0x3f803f80u, 0x3f803f80u}, P, z4);
    if (lane < 4) *(LAS float*)(L + L_L + lane * 32 + w * 4) = ls[0];
    STAMP(3);
    f32x4 O[8];
#pragma unroll
    for (int d = 0; d < 8; ++d) {
      const u32 sel = (d & 1) ? 0x07060302u : 0x05040100u;
      u32x4 a;
#pragma unroll
      for (int e = 0; e < 4; ++e) a[e] = __builtin_amdgcn_perm(V[2 * e + 1][d >> 1], V[2 * e][d >> 1], sel);
      O[d] = mfma(a, P, z4);
    }
    if (n < 4) {
#pragma unroll
      for (int d = 0; d < 8; ++d) *(LAS f32x4*)(L + L_OW + w * OW_STRIDE + n * 528 + g * 128 + d * 16) = O[d];
    }
    STAMP(4);
    bar();
    {
      const int h = w >> 1;
      LAS char* src = L + L_OW + h * 528 + (w & 1) * 256 + lane * 4;
      float r[8];
#pragma unroll
      for (int u = 0; u < 8; ++u) r[u] = *(LAS float*)(src + u * OW_STRIDE);
      const f32x4 la = *(LAS f32x4*)(L + L_L + h * 32), lb = *(LAS f32x4*)(L + L_L + h * 32 + 16);
      const float v = ((r[0] + r[1]) + (r[2] + r[3])) + ((r[4] + r[5]) + (r[6] + r[7]));
      const float lsum = ((la[0] + la[1]) + (la[2] + la[3])) + ((lb[0] + lb[1]) + (lb[2] + lb[3]));
      const u32 p01 = pk(v, dpp<SHL4>(v)), p23 = dppu<SHL8>(p01);
      const u32 mh = __builtin_amdgcn_readlane(__float_as_uint(m), h);
      const bool dat = (lane & 12) == 0;
      const u32 slot = dat ? (lane >> 5) * 8 + (lane & 3) * 2 + (g & 1) : 16 + (lane & 3) + (g & 1) * 4;
      if (dat || ((lane & 44) == 4))
        put(ws, WS_OP + ((xg * 8 + w) * 32 + b * 16 + s) * 384 + slot * 16, dat ? p01 : mh, dat ? p23 : __float_as_uint(lsum), tag);
    }
    STAMP(5);
    bar0();
    const int r = xs * 8 + (i >> 2);
    {
      u32x4 v[1];
      gather<1>(ws, WS_OP + (r * 32 + w * 4 + g) * 384 + n * 16, 0, tag, v);
      STAMP(6);
      *(LAS u32x2*)(L + L_OS + (w * 4 + g) * 144 + ((n >> 3) * 32 + (n & 3) * 8 + ((n >> 2) & 1) * 4) * 2) = u32x2{v[0].x, v[0].z};
    }
    bar();
    STAMP(7);
    {
      const int q = (lane >> 2) & 3, p = lane & 3;
      LAS char* a = L + L_WO + (4 * g + q) * 256 + ((w ^ (4 * (g & 1) + q)) * 32) + p * 8;
      LAS char* os = L + L_OS + n * 144 + g * 16;
      const u32x4 w0 = trd2(a, 4096), w1 = trd2(a + 8192, 4096), w2 = trd2(a + 16384, 4096);
      f32x4 a0 = mfma(*(LAS u32x4*)os, w0, z4), a1 = mfma(*(LAS u32x4*)(os + 16 * 144), w0, z4);
      a0 = mfma(*(LAS u32x4*)(os + 64), w1, a0);
      a1 = mfma(*(LAS u32x4*)(os + 16 * 144 + 64), w1, a1);
      const f32x4 ff = mfma(*(LAS u32x4*)(L + L_H + (n & 1) * 64 + g * 16), w2, z4);
      const f32x4 s0 = *(LAS f32x4*)(L + L_W + g * 16), s1 = *(LAS f32x4*)(L + L_W + 64 + g * 16), gg = *(LAS f32x4*)(L + L_G);
      float r0 = x32_sum(x16_sum((a0[0] * s0[0] + a0[1] * s0[1]) + (a0[2] * s0[2] + a0[3] * s0[3]))) * gg[0];
      float r1 = x32_sum(x16_sum((a1[0] * s1[0] + a1[1] * s1[1]) + (a1[2] * s1[2] + a1[3] * s1[3]))) * gg[2];
      if ((i >> 3) * 8 + xs < 28) { r0 = fmaf(gg[1], ff[0], r0); r1 = fmaf(gg[3], ff[1], r1); }
      const float rr = (g & 1) ? r1 : r0, rn = dpp<XOR1>(rr);
      if ((lane & 33) == 0) put(ws, WS_FP + ((c * 8 + w) * 2 + g) * 8192 + r * 128 + n * 8, __float_as_uint(rr), __float_as_uint(rn), tag);
    }
    STAMP(8);
  } else {
    const u32 vrow128 = wave * 32 + (lane >> 3), vch128 = ((lane & 7) ^ (2 * (g & 1))) * 16;
    {
      const char* base = wq + (size_t)c * 128 * 8192 + (xs * 512 + (i >> 2) * 64) * 2;
      const u32 va = vrow128 * 8192 + vch128, vb = (va ^ 64) + 8 * 8192;
      dma2(base, va, vb, lbase + L_T0 + wave * 4096);
      dma2(base, va + 16 * 8192, vb + 16 * 8192, lbase + L_T0 + wave * 4096 + 2048);
    }
    u32x4 xf[4];
#pragma unroll
    for (int ks = 0; ks < 4; ++ks) xf[ks] = *(const GAS u32x4*)((const GAS char*)x + (n & 1) * 1024 + c * 256 + ks * 64 + g * 16);
    const u32 tag = LOAD_TAG();
    asm volatile("s_waitcnt vmcnt(0)" : "+v"(xf[0]), "+v"(xf[1]), "+v"(xf[2]), "+v"(xf[3]) :: "memory");
    STAMP(1);
    bar0();
    proj_put(L + L_T0, xf, wave, lane, ws, WS_QP + (xs * 8 + c) * 4096 + ((i >> 2) * 32 + wave * 8) * 16, 16384, tag);
    STAMP(2);
    if (wave == 3) {
      const u32x4 x0 = *(const GAS u32x4*)((const GAS char*)x + lane * 16), x1 = *(const GAS u32x4*)((const GAS char*)x + 1024 + lane * 16),
                  gv = *(const GAS u32x4*)((const GAS char*)gw + lane * 16);
      float z0 = 0.f, z1 = 0.f;
#pragma unroll
      for (int e = 0; e < 4; ++e) {
        z0 += rbf(blo(x0[e]) * blo(gv[e])) + rbf(bhi(x0[e]) * bhi(gv[e]));
        z1 += rbf(blo(x1[e]) * blo(gv[e])) + rbf(bhi(x1[e]) * bhi(gv[e]));
      }
      z0 = x32_sum(x16_sum(row_sum(z0))); z1 = x32_sum(x16_sum(row_sum(z1)));
      const float z = rbf((lane & 1) ? z1 : z0);
      const float sg = rbf(__builtin_amdgcn_rcpf(1.f + __expf(-z)));
      if (lane < 2) *(LAS u32x2*)(L + L_G + lane * 8) = u32x2{__float_as_uint(sg), __float_as_uint(rbf(1.f - sg))};
    }
    bar0();
    {
      u32x4 v[4];
      gather<4>(ws, WS_QP + (xg * 2 + b) * 16384 + (wave * 64 + lane) * 16, 4096, tag, v);
      const float q0 = (__uint_as_float(v[0].x) + __uint_as_float(v[1].x)) + (__uint_as_float(v[2].x) + __uint_as_float(v[3].x));
      const float q1 = (__uint_as_float(v[0].z) + __uint_as_float(v[1].z)) + (__uint_as_float(v[2].z) + __uint_as_float(v[3].z));
      *(LAS u32*)(L + L_Q + wave * 272 + lane * 4) = pk(q0, q1);
    }
    STAMP(3);
    bar();
    bar0();
    const int gp = (i >> 3) * 8 + xg, gc = (i >> 3) * 8 + xs;
    if (gp < 28) {
      const char* base = ((i & 4) ? wu : wg) + (size_t)c * 128 * (F * 2) + gp * 128;
      const u32 va = vrow128 * (F * 2) + vch128, vb = (va ^ 64) + 8 * F * 2;
      dma2(base, va, vb, lbase + L_T1 + wave * 4096);
      dma2(base, va + 16 * F * 2, vb + 16 * F * 2, lbase + L_T1 + wave * 4096 + 2048);
    }
    {
      const u32 va = g * 1024 + (n ^ (2 * g)) * 16, vb = (va ^ 128) + 4096;
      const char* base = wo + (size_t)(xs * 512 + (i >> 2) * 64 + wave * 16) * 1024 + c * 256;
      dma2(base, va, vb, lbase + L_WO + wave * 4096);
      dma2(base, va + 8192, vb + 8192, lbase + L_WO + wave * 4096 + 2048);
      const int row = gc < 28 ? gc * 64 + ((i >> 2) & 1) * 32 + wave * 8 : 0;
      dma2(wd + (size_t)row * 1024 + c * 256, va, vb, lbase + L_WO + 16384 + wave * 2048);
    }
    STAMP(4);
    bar0();
    asm volatile("s_waitcnt vmcnt(6)" ::: "memory");
    STAMP(5);
    bar0();
    if (gp < 28) proj_put(L + L_T1, xf, wave, lane, ws, WS_HP + (gp * 2 + (wave >> 1)) * 4096 + (i & 7) * 512 + (wave & 1) * 128, 256, tag);
    STAMP(6);
    if (wave == 1) {
      if (gc < 28) {
        u32x4 v[4];
        gather<4>(ws, WS_HP + (gc * 2 + ((i >> 2) & 1)) * 4096 + lane * 16, 1024, tag, v);
        const float g0 = rbf(x32_sum(__uint_as_float(v[0].x) + __uint_as_float(v[1].x))), g1 = rbf(x32_sum(__uint_as_float(v[0].z) + __uint_as_float(v[1].z)));
        const float u0 = rbf(x32_sum(__uint_as_float(v[2].x) + __uint_as_float(v[3].x))), u1 = rbf(x32_sum(__uint_as_float(v[2].z) + __uint_as_float(v[3].z)));
        const float h0 = rbf(g0 * __builtin_amdgcn_rcpf(1.f + __expf(-g0))) * u0, h1 = rbf(g1 * __builtin_amdgcn_rcpf(1.f + __expf(-g1))) * u1;
        if (lane < 32) *(LAS u32*)(L + L_H + g * 64 + (((n >> 1) & 3) * 8 + (n >> 3) * 4 + (n & 1) * 2) * 2) = pk(h0, h1);
      }
    } else if (wave == 2) {
      u32x4 v[1];
      gather<1>(ws, WS_OP + ((xs * 8 + (i >> 2)) * 32 + (lane & 31)) * 384 + 256, 0, tag, v, lane < 32);
      const float ms = __uint_as_float(v[0].x), M = row_max(ms), e = __builtin_amdgcn_exp2f(ms - M);
      const float Ls = row_sum(e * __uint_as_float(v[0].z));
      if (lane < 32) *(LAS float*)(L + L_W + lane * 4) = e / Ls;
    }
    asm volatile("s_waitcnt vmcnt(0)" ::: "memory");
    STAMP(7);
    bar();
    if (wave == 3 && xg < 2) {
      const int f = xg * 32 + i;
      u32x4 v[8];
      gather<8>(ws, WS_FP + f * 8192 + lane * 16, 1024, tag, v);
      STAMP(8);
      float r0 = 0.f, r1 = 0.f;
#pragma unroll
      for (int u = 0; u < 8; ++u) { r0 += __uint_as_float(v[u].x); r1 += __uint_as_float(v[u].z); }
      r0 += dpp<ROR8>(r0); r1 += dpp<ROR8>(r1);
      r0 = x32_sum(x16_sum(r0)); r1 = x32_sum(x16_sum(r1));
      if (lane < 8) ((GAS u32*)out)[(f & 1) * 256 + (f >> 1) * 8 + lane] = pk(r0, r1);
      if (f == 0 && lane == 0) __builtin_amdgcn_raw_buffer_store_b32(tag, ws, 0, 0, 16);
      STAMP(9);
    }
  }
  STAMP_FLUSH;
}

__global__ void tiny(u64* p){ if(threadIdx.x==0) *p=__builtin_amdgcn_s_memrealtime(); }
extern "C" void kernel_launch(void* const* d_in, const int* in_sizes, int n_in, void* d_out, int out_size, void* d_ws, size_t ws_size,
                              hipStream_t stream) {
  Args a{d_in[0], d_in[1], d_in[2], d_in[3], d_in[4], d_in[6], d_in[7], d_in[8], d_in[9], d_out, d_ws, d_ws};
  tiny<<<1,64,0,stream>>>((u64*)((char*)d_ws+1024));
  fused_decode<<<256, 768, 0, stream>>>(a);
}
```

```cpp
#include <hip/hip_runtime.h>
#include <hip/hip_bf16.h>

typedef unsigned short u16;
typedef unsigned u32;
typedef unsigned long long u64;
typedef u32 u32x4 __attribute__((ext_vector_type(4)));
typedef u32 u32x2 __attribute__((ext_vector_type(2)));
typedef u32 u32x8 __attribute__((ext_vector_type(8)));
typedef u32 u32x16 __attribute__((ext_vector_type(16)));
typedef float f32x4 __attribute__((ext_vector_type(4)));
typedef short s16x4 __attribute__((ext_vector_type(4)));
typedef __bf16 bf16x8 __attribute__((ext_vector_type(8)));
typedef __bf16 bf16x2 __attribute__((ext_vector_type(2)));
#define LAS __attribute__((address_space(3)))
#define GAS __attribute__((address_space(1)))
#define CAS __attribute__((address_space(4)))

#define STAMP_DECL
#define STAMP(i)
#define STAMP_FLUSH
#define XSH 4
#define QSH 0
#define ROT (w + s + 3 * xg + 4 * b)
#define QSLEEP 24
#define ILV
#define LSLEEP 0
#define PSLEEP 1

namespace {
constexpr int D = 512, HKV = 8, SK = 16384, DH = 128, WIN = 4096, F = 1792;
constexpr u32 WS_QP = 4096;
constexpr u32 WS_HP = WS_QP + 8 * 2 * 4 * 4096;
constexpr u32 WS_OP = WS_HP + 32 * 2 * 4096;
constexpr u32 WS_FP = WS_OP + 64 * 32 * 384;
constexpr u32 WS_END = WS_FP + 64 * 8192;
constexpr u32 L_T0 = 0, L_T1 = 16384, L_WO = 32768  , L_OW = L_WO + 24576  , OW_STRIDE = 4 * 528;
constexpr u32 L_Q = L_OW + 8 * OW_STRIDE  , L_M = L_Q + 4 * 272  , L_F = L_M + 128  , L_SE = L_F + 16, L_L = L_SE,
              L_OS = L_L + 128  , L_H = L_OS + 32 * 144  , L_W = L_H + 128  ,
              L_G = L_W + 128, L_X = L_G + 16, L_END = L_X + 4096;
constexpr int SPIN_CAP = 1 << 16;

__device__ __forceinline__ f32x4 mfma(u32x4 a, u32x4 b, f32x4 c) {
  return __builtin_amdgcn_mfma_f32_16x16x32_bf16(__builtin_bit_cast(bf16x8, a), __builtin_bit_cast(bf16x8, b), c, 0, 0, 0);
}
__device__ __forceinline__ u32 pk(float lo, float hi) {
  bf16x2 v = {(__bf16)lo, (__bf16)hi};
  return __builtin_bit_cast(u32, v);
}
__device__ __forceinline__ float rbf(float f) { return (float)(__bf16)f; }
__device__ __forceinline__ float blo(u32 v) { return __uint_as_float(v << 16); }
__device__ __forceinline__ float bhi(u32 v) { return __uint_as_float(v & 0xffff0000u); }
template <int CTRL> __device__ __forceinline__ float dpp(float x) {
  return __builtin_bit_cast(float, __builtin_amdgcn_mov_dpp(__builtin_bit_cast(int, x), CTRL, 0xf, 0xf, true));
}
template <int CTRL> __device__ __forceinline__ u32 dppu(u32 x) { return (u32)__builtin_amdgcn_mov_dpp((int)x, CTRL, 0xf, 0xf, true); }
constexpr int ROR1 = 0x121, ROR2 = 0x122, ROR4 = 0x124, ROR8 = 0x128, SHL4 = 0x104, SHL8 = 0x108, XOR1 = 0xB1;
__device__ __forceinline__ float row_sum(float x) { x += dpp<ROR8>(x); x += dpp<ROR4>(x); x += dpp<ROR2>(x); x += dpp<ROR1>(x); return x; }
__device__ __forceinline__ float row_max(float x) {
  x = fmaxf(x, dpp<ROR8>(x)); x = fmaxf(x, dpp<ROR4>(x)); x = fmaxf(x, dpp<ROR2>(x)); x = fmaxf(x, dpp<ROR1>(x)); return x;
}
__device__ __forceinline__ float x16_sum(float x) {
  auto s = __builtin_amdgcn_permlane16_swap(__float_as_uint(x), __float_as_uint(x), false, false);
  return __uint_as_float(s[0]) + __uint_as_float(s[1]);
}
__device__ __forceinline__ float x32_sum(float x) {
  auto s = __builtin_amdgcn_permlane32_swap(__float_as_uint(x), __float_as_uint(x), false, false);
  return __uint_as_float(s[0]) + __uint_as_float(s[1]);
}
__device__ __forceinline__ float x16_32_max(float x) {
  auto s = __builtin_amdgcn_permlane16_swap(__float_as_uint(x), __float_as_uint(x), false, false);
  x = fmaxf(__uint_as_float(s[0]), __uint_as_float(s[1]));
  auto t = __builtin_amdgcn_permlane32_swap(__float_as_uint(x), __float_as_uint(x), false, false);
  return fmaxf(__uint_as_float(t[0]), __uint_as_float(t[1]));
}
__device__ __forceinline__ void bar() { asm volatile("s_waitcnt lgkmcnt(0)\n\ts_barrier" ::: "memory"); }
__device__ __forceinline__ void bar0() { asm volatile("s_barrier" ::: "memory"); }
__device__ __forceinline__ void put(__amdgpu_buffer_rsrc_t ws, u32 off, u32 a, u32 b, u32 tag) {
  __builtin_amdgcn_raw_buffer_store_b128(u32x4{a, tag, b, tag}, ws, off, 0, 16);
}
__device__ __forceinline__ u32x4 get(__amdgpu_buffer_rsrc_t ws, u32 off) { return __builtin_amdgcn_raw_buffer_load_b128(ws, off, 0, 16); }
__device__ __forceinline__ bool fresh(u32x4 v, u32 tag) { return (v.y == tag) & (v.w == tag); }
template <int N> __device__ __forceinline__ void gather(__amdgpu_buffer_rsrc_t ws, u32 off, u32 stride, u32 tag, u32x4 (&v)[N], bool need = true) {
#pragma unroll
  for (int k = 0; k < N; ++k) v[k] = u32x4{0, 0, 0, 0};
#pragma unroll 1
  for (int spin = 0; spin < SPIN_CAP; ++spin) {
    asm volatile("" ::: "memory");
    if (need) {
      bool ok = true;
#pragma unroll
      for (int k = 0; k < N; ++k) v[k] = get(ws, off + (stride ? k * stride : (k >> 1) * 4096 + (k & 1) * 1024));
#pragma unroll
      for (int k = 0; k < N; ++k) ok &= fresh(v[k], tag);
      need = !ok;
    }
    if (!__any(need)) break;
    __builtin_amdgcn_s_sleep(PSLEEP);
  }
}
constexpr u32 EMPTY = 0xffffffffu;
__device__ __forceinline__ bool full(u32x4 v) { return (v.x != EMPTY) & (v.y != EMPTY) & (v.z != EMPTY) & (v.w != EMPTY); }
__device__ __forceinline__ u32x4 lds_wait(LAS char* p) {
  u32x4 v;
#pragma unroll 1
  for (int spin = 0; spin < SPIN_CAP; ++spin) {
    v = *(volatile LAS u32x4*)p;
    if (__all(full(v))) break;
    if (LSLEEP) __builtin_amdgcn_s_sleep(LSLEEP);
  }
  asm volatile("" ::: "memory");
  return v;
}
__device__ __forceinline__ void dma2(const char* base, u32 va, u32 vb, u32 lds) {
  asm volatile("s_mov_b32 m0, %2\n\ts_nop 0\n\tglobal_load_lds_dwordx4 %0, %3 nt\n\ts_add_u32 m0, m0, 0x400\n\ts_nop 0\n\tglobal_load_lds_dwordx4 %1, %3 nt"
               :: "v"(va), "v"(vb), "s"(lds), "s"(base) : "memory", "scc");
}
__device__ __forceinline__ u32x2 trd(LAS char* p) {
  return __builtin_bit_cast(u32x2, __builtin_amdgcn_ds_read_tr16_b64_v4i16((LAS s16x4*)p));
}
__device__ __forceinline__ u32x4 trd2(LAS char* p, int d) { const u32x2 lo = trd(p), hi = trd(p + d); return u32x4{lo.x, lo.y, hi.x, hi.y}; }
__device__ __forceinline__ void proj_put(LAS char* tile, const u32x4 (&xf)[4], int t, int lane, __amdgpu_buffer_rsrc_t ws, u32 off, u32 bstride, u32 tag) {
  const int g = lane >> 4, q = (lane >> 2) & 3, p = lane & 3, f = (q >> 1) | ((g & 1) << 1);
  LAS char* a = tile + (8 * g + q) * 128 + ((t ^ f) * 32) + p * 8;
  const f32x4 z = {0.f, 0.f, 0.f, 0.f};
  f32x4 c0 = mfma(trd2(a, 512), xf[0], z), c1 = mfma(trd2(a + 4096, 512), xf[1], z);
  c0 = mfma(trd2(a + 8192, 512), xf[2], c0);
  c1 = mfma(trd2(a + 12288, 512), xf[3], c1);
  c0 += c1;
  const bool up = lane & 2;
  if ((lane & 12) == 0)
    put(ws, off + (lane & 1) * bstride + (g * 2 + ((lane >> 1) & 1)) * 16, __float_as_uint(up ? c0[2] : c0[0]), __float_as_uint(up ? c0[3] : c0[1]), tag);
}
}

extern "C" __global__ void __launch_bounds__(768, 3) fused_decode(const char* wq, const char* x, const char* kc, const char* vc, char* wsp, const char* gw, const char* wo, const char* wg, const char* wu, const char* wd, char* out) {
  __shared__ __attribute__((aligned(1024))) char smem[L_END];
  LAS char* L = (LAS char*)smem;
  const u32 lbase = (u32)(size_t)L;
  STAMP_DECL;
  STAMP(0);
  STAMP(10);
  const int j = blockIdx.x, xg = j & 7, i = j >> 3, xs = (xg + XSH) & 7, xq = (xg + QSH) & 7;
  const int wave = __builtin_amdgcn_readfirstlane(threadIdx.x >> 6), lane = threadIdx.x & 63;
  const int g = lane >> 4, n = lane & 15;
  const int b = i & 1, s = i >> 1, c = i & 3;
  const __amdgpu_buffer_rsrc_t ws = __builtin_amdgcn_make_buffer_rsrc(wsp, 0, WS_END, 0x00020000);
  const f32x4 z4 = {0.f, 0.f, 0.f, 0.f};
#define LOAD_TAG() (*(const CAS u32*)wsp * 1664525u + 1013904223u)

  if (wave >= 4) {
    const int w = wave - 4;
    const size_t kvoff = ((size_t)(b * HKV + xg) * SK + (SK - WIN) + s * 256 + ((ROT) & 7) * 32) * (DH * 2) + 4096;
    const GAS char* kp = (const GAS char*)kc + kvoff;
    const GAS char* vp = (const GAS char*)vc + kvoff;
    const u32 ko = (n >> 2) * 256 + g * 64 + (n & 3) * 16, vo = lane * 16;
    kp -= ((ROT) & 7) * 8192 + 4096; vp -= ((ROT) & 7) * 8192 + 4096;
    const int rot = (j * 37 + i * 11) + w;
#define KVOFF(u) ((((u) * 8 + rot) & 63) * 1024)
    u32x4 K[8], V[8];
    if (w < 2 && threadIdx.x - 256 < (L_SE - L_Q) / 16) *(LAS u32x4*)(L + L_Q + (threadIdx.x - 256) * 16) = u32x4{EMPTY, EMPTY, EMPTY, EMPTY};
    __builtin_amdgcn_s_setprio(2);
    bar();
#pragma unroll
    for (int u = 0; u < 8; ++u) K[u] = __builtin_nontemporal_load((const GAS u32x4*)(kp + ko + KVOFF(u)));
    __builtin_amdgcn_sched_barrier(0);
    __builtin_amdgcn_s_setprio(1);
    STAMP(9);
#pragma unroll
    for (int u = 0; u < 8; ++u) V[u] = __builtin_nontemporal_load((const GAS u32x4*)(vp + vo + KVOFF(u)));
    __builtin_amdgcn_sched_barrier(0);
    const u32 tag = LOAD_TAG();
    STAMP(1);
    const int hd = n >> 2;
    const u32x4 qf = lds_wait(L + L_Q + hd * 272 + g * 64 + (n & 3) * 16);
    STAMP(2);
    float S[8];
#pragma unroll
    for (int u = 0; u < 8; ++u) {
      const f32x4 d = mfma(K[u], qf, z4);
      S[u] = (dpp<0x00>(d[0]) + dpp<0x55>(d[1])) + (dpp<0xAA>(d[2]) + dpp<0xFF>(d[3]));
    }
    constexpr float C2 = 0.08838834764831845f * 1.4426950408889634f;
    float m = fmaxf(fmaxf(fmaxf(S[0], S[1]), fmaxf(S[2], S[3])), fmaxf(fmaxf(S[4], S[5]), fmaxf(S[6], S[7])));
    m = x16_32_max(m);
    if ((lane & 51) == 0) *(LAS float*)(L + L_M + hd * 32 + w * 4) = m;
    {
      u32x4 wa, wb;
#pragma unroll 1
      for (int spin = 0; spin < SPIN_CAP; ++spin) {
        wa = *(volatile LAS u32x4*)(L + L_M + hd * 32); wb = *(volatile LAS u32x4*)(L + L_M + hd * 32 + 16);
        if (__all(full(wa) & full(wb))) break;
      }
      const f32x4 ma = __builtin_bit_cast(f32x4, wa), mb = __builtin_bit_cast(f32x4, wb);
      m = fmaxf(fmaxf(fmaxf(ma[0], ma[1]), fmaxf(ma[2], ma[3])), fmaxf(fmaxf(mb[0], mb[1]), fmaxf(mb[2], mb[3]))) * C2;
    }
    u32x4 P;
    P.x = pk(__builtin_amdgcn_exp2f(fmaf(S[0], C2, -m)), __builtin_amdgcn_exp2f(fmaf(S[1], C2, -m)));
    P.y = pk(__builtin_amdgcn_exp2f(fmaf(S[2], C2, -m)), __builtin_amdgcn_exp2f(fmaf(S[3], C2, -m)));
    P.z = pk(__builtin_amdgcn_exp2f(fmaf(S[4], C2, -m)), __builtin_amdgcn_exp2f(fmaf(S[5], C2, -m)));
    P.w = pk(__builtin_amdgcn_exp2f(fmaf(S[6], C2, -m)), __builtin_amdgcn_exp2f(fmaf(S[7], C2, -m)));
    const f32x4 ls = mfma(u32x4{0x3f803f80u, 0x3f803f80u, 0x3f803f80u, 0x3f803f80u}, P, z4);
    if ((lane & 51) == 0) *(LAS float*)(L + L_L + hd * 32 + w * 4) = ls[0];
    STAMP(3);
    f32x4 O[8];
#pragma unroll
    for (int d = 0; d < 8; ++d) {
      const u32 sel = (d & 1) ? 0x07060302u : 0x05040100u;
      u32x4 a;
#pragma unroll
      for (int e = 0; e < 4; ++e) a[e] = __builtin_amdgcn_perm(V[2 * e + 1][d >> 1], V[2 * e][d >> 1], sel);
      O[d] = mfma(a, P, z4);
    }
    if ((n & 3) == 0) {
#pragma unroll
      for (int d = 0; d < 8; ++d) *(LAS f32x4*)(L + L_OW + w * OW_STRIDE + hd * 528 + g * 128 + d * 16) = O[d];
    }
    STAMP(4);
    bar();
    {
      const int h = w >> 1;
      LAS char* src = L + L_OW + h * 528 + (w & 1) * 256 + lane * 4;
      float r[8];
#pragma unroll
      for (int u = 0; u < 8; ++u) r[u] = *(LAS float*)(src + u * OW_STRIDE);
      const f32x4 la = *(LAS f32x4*)(L + L_L + h * 32), lb = *(LAS f32x4*)(L + L_L + h * 32 + 16);
      const float v = ((r[0] + r[1]) + (r[2] + r[3])) + ((r[4] + r[5]) + (r[6] + r[7]));
      const float lsum = ((la[0] + la[1]) + (la[2] + la[3])) + ((lb[0] + lb[1]) + (lb[2] + lb[3]));
      const u32 p01 = pk(v, dpp<SHL4>(v)), p23 = dppu<SHL8>(p01);
      const u32 mh = __builtin_amdgcn_readlane(__float_as_uint(m), 4 * h);
      const bool dat = (lane & 12) == 0;
      const u32 slot = dat ? (lane >> 5) * 8 + (lane & 3) * 2 + (g & 1) : 16 + (lane & 3) + (g & 1) * 4;
      if (dat || ((lane & 44) == 4))
        put(ws, WS_OP + ((xg * 8 + w) * 32 + b * 16 + s) * 384 + slot * 16, dat ? p01 : mh, dat ? p23 : __float_as_uint(lsum), tag);
    }
    STAMP(5);
    bar0();
    const int r = xs * 8 + (i >> 2);
    {
      u32x4 v[1];
      gather<1>(ws, WS_OP + (r * 32 + w * 4 + g) * 384 + n * 16, 0, tag, v);
      STAMP(6);
      *(LAS u32x2*)(L + L_OS + (w * 4 + g) * 144 + ((n >> 3) * 32 + (n & 3) * 8 + ((n >> 2) & 1) * 4) * 2) = u32x2{v[0].x, v[0].z};
    }
    bar();
    STAMP(7);
    {
      const int q = (lane >> 2) & 3, p = lane & 3;
      LAS char* a = L + L_WO + (4 * g + q) * 256 + ((w ^ (4 * (g & 1) + q)) * 32) + p * 8;
      LAS char* os = L + L_OS + n * 144 + g * 16;
      const u32x4 w0 = trd2(a, 4096), w1 = trd2(a + 8192, 4096), w2 = trd2(a + 16384, 4096);
      f32x4 a0 = mfma(*(LAS u32x4*)os, w0, z4), a1 = mfma(*(LAS u32x4*)(os + 16 * 144), w0, z4);
      a0 = mfma(*(LAS u32x4*)(os + 64), w1, a0);
      a1 = mfma(*(LAS u32x4*)(os + 16 * 144 + 64), w1, a1);
      const f32x4 ff = mfma(*(LAS u32x4*)(L + L_H + (n & 1) * 64 + g * 16), w2, z4);
      const f32x4 s0 = *(LAS f32x4*)(L + L_W + g * 16), s1 = *(LAS f32x4*)(L + L_W + 64 + g * 16), gg = *(LAS f32x4*)(L + L_G);
      float r0 = x32_sum(x16_sum((a0[0] * s0[0] + a0[1] * s0[1]) + (a0[2] * s0[2] + a0[3] * s0[3]))) * gg[0];
      float r1 = x32_sum(x16_sum((a1[0] * s1[0] + a1[1] * s1[1]) + (a1[2] * s1[2] + a1[3] * s1[3]))) * gg[2];
      if ((i >> 3) * 8 + xs < 28) { r0 = fmaf(gg[1], ff[0], r0); r1 = fmaf(gg[3], ff[1], r1); }
      const float rr = (g & 1) ? r1 : r0, rn = dpp<XOR1>(rr);
      if ((lane & 33) == 0) put(ws, WS_FP + ((c * 8 + w) * 2 + g) * 8192 + r * 128 + n * 8, __float_as_uint(rr), __float_as_uint(rn), tag);
    }
    STAMP(8);
  } else {
    const u32 vrow128 = wave * 32 + (lane >> 3), vch128 = ((lane & 7) ^ (2 * (g & 1))) * 16;
    {
      const char* base = wq + (size_t)c * 128 * 8192 + (xq * 512 + (i >> 2) * 64) * 2;
      const u32 va = vrow128 * 8192 + vch128, vb = (va ^ 64) + 8 * 8192;
      dma2(base, va, vb, lbase + L_T0 + wave * 4096);
      dma2(base, va + 16 * 8192, vb + 16 * 8192, lbase + L_T0 + wave * 4096 + 2048);
    }
    asm volatile("s_mov_b32 m0, %1\n\ts_nop 0\n\tglobal_load_lds_dwordx4 %0, %2"
                 :: "v"((g & 1) * 1024 + c * 256 + n * 16), "s"(lbase + L_X + wave * 1024), "s"(x) : "memory");
    u32x4 x0, x1, gv;
    if (wave == 3) {
      x0 = *(const GAS u32x4*)((const GAS char*)x + lane * 16); x1 = *(const GAS u32x4*)((const GAS char*)x + 1024 + lane * 16);
      gv = *(const GAS u32x4*)((const GAS char*)gw + lane * 16);
    }
    __builtin_amdgcn_sched_barrier(0);
    __builtin_amdgcn_s_setprio(3);
    bar0();
    const u32 tag = LOAD_TAG();
    __builtin_amdgcn_s_waitcnt(0x0F70);
    asm volatile("s_waitcnt vmcnt(0)" ::: "memory");
    if (lane == 0) *(volatile LAS u32*)(L + L_F + wave * 4) = 0;
    u32x4 xf[4];
#pragma unroll
    for (int ks = 0; ks < 4; ++ks) xf[ks] = *(LAS u32x4*)(L + L_X + wave * 1024 + (n & 1) * 256 + ks * 64 + g * 16);
    STAMP(1);
    lds_wait(L + L_F);
    proj_put(L + L_T0, xf, wave, lane, ws, WS_QP + (xq * 8 + c) * 4096 + ((i >> 2) * 32 + wave * 8) * 16, 16384, tag);
    STAMP(2);
    if (wave == 3) {
      float z0 = 0.f, z1 = 0.f;
#pragma unroll
      for (int e = 0; e < 4; ++e) {
        z0 += rbf(blo(x0[e]) * blo(gv[e])) + rbf(bhi(x0[e]) * bhi(gv[e]));
        z1 += rbf(blo(x1[e]) * blo(gv[e])) + rbf(bhi(x1[e]) * bhi(gv[e]));
      }
      z0 = x32_sum(x16_sum(row_sum(z0))); z1 = x32_sum(x16_sum(row_sum(z1)));
      const float z = rbf((lane & 1) ? z1 : z0);
      const float sg = rbf(__builtin_amdgcn_rcpf(1.f + __expf(-z)));
      if (lane < 2) *(LAS u32x2*)(L + L_G + lane * 8) = u32x2{__float_as_uint(sg), __float_as_uint(rbf(1.f - sg))};
    }
    const int gp = (i >> 3) * 8 + xg, gc = (i >> 3) * 8 + xs;
    if (wave < 2) {
      __builtin_amdgcn_s_sleep(QSLEEP);
      u32x4 v[8];
      gather<8>(ws, WS_QP + (xg * 2 + b) * 16384 + (wave * 128 + lane) * 16, 0, tag, v);
#pragma unroll
      for (int e = 0; e < 2; ++e) {
        const float q0 = (__uint_as_float(v[e].x) + __uint_as_float(v[2 + e].x)) + (__uint_as_float(v[4 + e].x) + __uint_as_float(v[6 + e].x));
        const float q1 = (__uint_as_float(v[e].z) + __uint_as_float(v[2 + e].z)) + (__uint_as_float(v[4 + e].z) + __uint_as_float(v[6 + e].z));
        *(LAS u32*)(L + L_Q + (wave * 2 + e) * 272 + lane * 4) = pk(q0, q1);
      }
      STAMP(3);
    } else {
      __builtin_amdgcn_s_setprio(0);
      const int v2 = wave & 1;
      if (gp < 28) {
        const char* base = ((i & 4) ? wu : wg) + (size_t)(c * 128 + v2 * 64) * (F * 2) + gp * 128;
        const u32 va = (lane >> 3) * (F * 2) + vch128, vb = (va ^ 64) + 8 * F * 2;
#pragma unroll
        for (int e = 0; e < 4; ++e) dma2(base, va + e * 16 * F * 2, vb + e * 16 * F * 2, lbase + L_T1 + v2 * 8192 + e * 2048);
      }
      const u32 va = g * 1024 + (n ^ (2 * g)) * 16, vb = (va ^ 128) + 4096;
      const char* base = wo + (size_t)(xs * 512 + (i >> 2) * 64 + v2 * 32) * 1024 + c * 256;
#pragma unroll
      for (int e = 0; e < 4; ++e) dma2(base, va + e * 8192, vb + e * 8192, lbase + L_WO + v2 * 8192 + e * 2048);
      const int row = gc < 28 ? gc * 64 + ((i >> 2) & 1) * 32 + v2 * 16 : 0;
#pragma unroll
      for (int e = 0; e < 2; ++e) dma2(wd + (size_t)row * 1024 + c * 256, va + e * 8192, vb + e * 8192, lbase + L_WO + 16384 + v2 * 4096 + e * 2048);
      __builtin_amdgcn_s_setprio(3);
    }
    STAMP(4);
    bar0();
    asm volatile("s_waitcnt vmcnt(12)" ::: "memory");
    STAMP(5);
    bar0();
    if (gp < 28) proj_put(L + L_T1, xf, wave, lane, ws, WS_HP + (gp * 2 + (wave >> 1)) * 4096 + (i & 7) * 512 + (wave & 1) * 128, 256, tag);
    STAMP(6);
    if (wave == 1) {
      if (gc < 28) {
        u32x4 v[4];
        gather<4>(ws, WS_HP + (gc * 2 + ((i >> 2) & 1)) * 4096 + lane * 16, 1024, tag, v);
        const float g0 = rbf(x32_sum(__uint_as_float(v[0].x) + __uint_as_float(v[1].x))), g1 = rbf(x32_sum(__uint_as_float(v[0].z) + __uint_as_float(v[1].z)));
        const float u0 = rbf(x32_sum(__uint_as_float(v[2].x) + __uint_as_float(v[3].x))), u1 = rbf(x32_sum(__uint_as_float(v[2].z) + __uint_as_float(v[3].z)));
        const float h0 = rbf(g0 * __builtin_amdgcn_rcpf(1.f + __expf(-g0))) * u0, h1 = rbf(g1 * __builtin_amdgcn_rcpf(1.f + __expf(-g1))) * u1;
        if (lane < 32) *(LAS u32*)(L + L_H + g * 64 + (((n >> 1) & 3) * 8 + (n >> 3) * 4 + (n & 1) * 2) * 2) = pk(h0, h1);
      }
    } else if (wave == 2) {
      u32x4 v[1];
      gather<1>(ws, WS_OP + ((xs * 8 + (i >> 2)) * 32 + (lane & 31)) * 384 + 256, 0, tag, v, lane < 32);
      const float ms = __uint_as_float(v[0].x), M = row_max(ms), e = __builtin_amdgcn_exp2f(ms - M);
      const float Ls = row_sum(e * __uint_as_float(v[0].z));
      if (lane < 32) *(LAS float*)(L + L_W + lane * 4) = e / Ls;
    }
    asm volatile("s_waitcnt vmcnt(0)" ::: "memory");
    STAMP(7);
    bar();
    if (wave == 3 && xg < 2) {
      const int f = xg * 32 + i;
      u32x4 v[8];
      gather<8>(ws, WS_FP + f * 8192 + lane * 16, 1024, tag, v);
      STAMP(8);
      float r0 = 0.f, r1 = 0.f;
#pragma unroll
      for (int u = 0; u < 8; ++u) { r0 += __uint_as_float(v[u].x); r1 += __uint_as_float(v[u].z); }
      r0 += dpp<ROR8>(r0); r1 += dpp<ROR8>(r1);
      r0 = x32_sum(x16_sum(r0)); r1 = x32_sum(x16_sum(r1));
      if (lane < 8) ((GAS u32*)out)[(f & 1) * 256 + (f >> 1) * 8 + lane] = pk(r0, r1);
      if (f == 0 && lane == 0) __builtin_amdgcn_raw_buffer_store_b32(tag, ws, 0, 0, 16);
      STAMP(9);
    }
  }
  STAMP_FLUSH;
}

extern "C" void kernel_launch(void* const* d_in, const int* in_sizes, int n_in, void* d_out, int out_size, void* d_ws, size_t ws_size,
                              hipStream_t stream) {
  fused_decode<<<256, 768, 0, stream>>>((const char*)d_in[3], (const char*)d_in[0], (const char*)d_in[1], (const char*)d_in[2], (char*)d_ws, (const char*)d_in[9], (const char*)d_in[4], (const char*)d_in[6], (const char*)d_in[7], (const char*)d_in[8], (char*)d_out);
}
```

```cpp
#include <hip/hip_runtime.h>

struct Args { const void *x, *kc, *vc, *wq; void* ws; const void *wg, *wu, *gw, *wo, *wd; void *out, *ws2; };

__global__ void __launch_bounds__(768, 3) fused_decode(Args) {
  __shared__ __attribute__((aligned(1024))) char smem[82944];
  asm volatile(
    ".Lfd_ENTRY:\n"
    "s_load_dwordx16 s[4:19], s[0:1], 0x0\n"
    "s_load_dwordx8 s[20:27], s[0:1], 0x40\n"
    "s_cmp_lt_u32 s2, 16\n"
    "s_cbranch_scc0 .Lfd_NOPF\n"
    "v_lshlrev_b32 v1, 7, v0\n"
    "v_cmp_gt_u32 vcc, .Lfd_CODE_END-.Lfd_PFBASE-1024, v1\n"
    "s_and_b64 exec, exec, vcc\n"
    "s_cbranch_execz .Lfd_PFEND\n"
    "s_getpc_b64 s[38:39]\n"
    ".Lfd_PFBASE:\n"
    "global_load_dword v95, v1, s[38:39] offset:1024\n"
    ".Lfd_PFEND:\n"
    "s_mov_b64 exec, -1\n"
    ".Lfd_NOPF:\n"
    "v_readfirstlane_b32 s28, v0\n"
    "v_and_b32 v1, 63, v0\n"
    "s_and_b32 s29, s2, 7\n"
    "s_lshr_b32 s30, s2, 3\n"
    "s_lshr_b32 s28, s28, 6\n"
    "s_and_b32 s31, s30, 3\n"
    "s_lshr_b32 s34, s2, 5\n"
    "s_cmp_ge_u32 s28, 4\n"
    "s_cbranch_scc1 .Lfd_KV\n"
    "v_lshrrev_b32 v5, 3, v1\n"
    "v_and_b32 v6, 2, v5\n"
    "v_and_b32 v2, 7, v1\n"
    "v_xor_b32 v2, v6, v2\n"
    "v_lshlrev_b32 v2, 4, v2\n"
    "v_lshl_or_b32 v3, v5, 13, v2\n"
    "s_add_u32 s46, s29, 4\n"
    "s_and_b32 s46, s46, 7\n"
    "s_lshl_b32 s47, s31, 2\n"
    "s_add_u32 s47, s47, s28\n"
    "s_lshl_b32 s47, s47, 18\n"
    "s_lshl3_add_u32 s48, s46, s34\n"
    "s_lshl_b32 s48, s48, 7\n"
    "s_add_u32 s47, s47, s48\n"
    "s_lshl_b32 m0, s28, 12\n"
    "v_xor_b32 v4, 0x10040, v3\n"
    "v_xor_b32 v5, 0x20000, v3\n"
    "v_xor_b32 v6, 0x30040, v3\n"
    "v_and_b32 v7, 16, v1\n"
    "v_and_b32 v8, 15, v1\n"
    "v_lshlrev_b32 v8, 4, v8\n"
    "v_lshl_or_b32 v7, v7, 6, v8\n"
    "s_lshl_b32 s48, s31, 8\n"
    "s_setprio 3\n"
    "s_waitcnt lgkmcnt(0)\n"
    "s_add_u32 s38, s10, s47\n"
    "s_addc_u32 s39, s11, 0\n"
    "s_cmp_lg_u32 s28, 0\n"
    "s_cbranch_scc1 .Lfd_NOX1\n"
    "s_add_u32 s40, s4, s48\n"
    "s_addc_u32 s41, s5, 0\n"
    "s_mov_b32 m0, 57344\n"
    "s_nop 0\n"
    "global_load_lds_dwordx4 v7, s[40:41]\n"
    "s_mov_b32 m0, 0\n"
    ".Lfd_NOX1:\n"
    "s_nop 0\n"
    "global_load_lds_dwordx4 v3, s[38:39] nt\n"
    "s_add_u32 m0, m0, 0x400\n"
    "s_nop 0\n"
    "global_load_lds_dwordx4 v4, s[38:39] nt\n"
    "s_add_u32 m0, m0, 0x400\n"
    "s_nop 0\n"
    "global_load_lds_dwordx4 v5, s[38:39] nt\n"
    "s_add_u32 m0, m0, 0x400\n"
    "s_nop 0\n"
    "global_load_lds_dwordx4 v6, s[38:39] nt\n"
    "s_barrier\n"
    "s_branch .Lfd_SMAIN\n"
    ".Lfd_KV:\n"
    "s_sub_u32 s53, s28, 4\n"
    "v_lshlrev_b32 v117, 4, v0\n"
    "v_add_u32 v117, 54272, v117\n"
    "v_mov_b32 v4, 2139095040\n"
    "v_mov_b32 v5, v4\n"
    "v_mov_b32 v6, v4\n"
    "v_mov_b32 v7, v4\n"
    "ds_write_b128 v117, v[4:7]\n"
    "ds_write_b128 v117, v[4:7] offset:8192\n"
    "ds_write_b128 v117, v[4:7] offset:16384\n"
    "v_and_b32 v8, 12, v1\n"
    "v_and_b32 v9, 48, v1\n"
    "v_and_b32 v10, 3, v1\n"
    "v_lshlrev_b32 v10, 4, v10\n"
    "v_lshl_or_b32 v10, v8, 6, v10\n"
    "v_lshl_or_b32 v116, v9, 2, v10\n"
    "s_mul_i32 s46, s2, 37\n"
    "s_mul_i32 s47, s30, 11\n"
    "s_add_u32 s46, s46, s47\n"
    "s_add_u32 s46, s46, s53\n"
    "s_and_b32 s46, s46, 63\n"
    "s_lshl_b32 s46, s46, 10\n"
    "v_or_b32 v116, s46, v116\n"
    "v_lshl_or_b32 v117, v1, 4, s46\n"
    "s_and_b32 s47, s30, 1\n"
    "s_lshl3_add_u32 s47, s47, s29\n"
    "s_lshl_b32 s47, s47, 22\n"
    "s_lshr_b32 s48, s30, 1\n"
    "s_lshl_b32 s48, s48, 16\n"
    "s_add_u32 s47, s47, s48\n"
    "s_add_u32 s47, s47, 0x300000\n"
    "s_movk_i32 s50, 0x2000\n"
    "s_waitcnt lgkmcnt(0)\n"
    "s_add_u32 s60, s6, s47\n"
    "s_addc_u32 s61, s7, 0\n"
    "s_add_u32 s62, s8, s47\n"
    "s_addc_u32 s63, s9, 0\n"
    "s_lshl3_add_u32 s48, s53, s34\n"
    "s_cmp_lg_u32 s48, 0\n"
    "s_cbranch_scc1 .Lfd_NOTLB\n"
    "s_load_dword s88, s[60:61], 0\n"
    ".Lfd_NOTLB:\n"
    "s_barrier\n"
    "s_sleep 8\n"
    "global_load_dwordx4 v[32:35], v116, s[60:61] nt\n"
    "v_add_u16 v116, s50, v116\n"
    "global_load_dwordx4 v[36:39], v116, s[60:61] nt\n"
    "v_add_u16 v116, s50, v116\n"
    "global_load_dwordx4 v[40:43], v116, s[60:61] nt\n"
    "v_add_u16 v116, s50, v116\n"
    "global_load_dwordx4 v[44:47], v116, s[60:61] nt\n"
    "v_add_u16 v116, s50, v116\n"
    "s_load_dword s56, s[12:13], 0\n"
    "s_lshl_b32 s46, s53, 2\n"
    "s_and_b32 s57, s29, 3\n"
    "s_andn2_b32 s35, 4, s29\n"
    "s_lshl3_add_u32 s35, s35, s30\n"
    "v_lshrrev_b32 v16, 4, v1\n"
    "v_and_b32 v17, 15, v1\n"
    "v_lshrrev_b32 v18, 2, v17\n"
    "v_and_b32 v19, 3, v1\n"
    "v_mul_u32_u24 v2, 272, v18\n"
    "v_lshl_add_u32 v2, v16, 6, v2\n"
    "v_lshl_add_u32 v2, v19, 4, v2\n"
    "v_lshlrev_b32 v3, 5, v18\n"
    "v_add_u32 v4, s46, v3\n"
    "v_add_u32 v4, 59456, v4\n"
    "s_mul_i32 s47, s53, 2176\n"
    "s_add_u32 s47, s47, 60096\n"
    "v_lshl_add_u32 v5, v1, 4, s47\n"
    "s_lshr_b32 s47, s53, 1\n"
    "s_and_b32 s48, s53, 1\n"
    "s_lshl_b32 s48, s48, 9\n"
    "s_lshl_b32 s49, s47, 6\n"
    "s_add_u32 s48, s48, s49\n"
    "s_add_u32 s48, s48, 60096\n"
    "v_and_b32 v20, 1, v16\n"
    "v_mul_u32_u24 v6, 1088, v20\n"
    "v_lshrrev_b32 v21, 5, v1\n"
    "v_lshl_add_u32 v6, v21, 8, v6\n"
    "v_lshl_add_u32 v6, v17, 2, v6\n"
    "v_add_u32 v6, s48, v6\n"
    "s_lshl_b32 s49, s47, 5\n"
    "s_add_u32 s58, s49, 59712\n"
    "s_lshl_b32 s59, s47, 2\n"
    "v_lshrrev_b32 v22, 2, v1\n"
    "v_lshl_add_u32 v22, v19, 4, v22\n"
    "v_lshlrev_b32 v7, 4, v22\n"
    "v_mul_u32_u24 v8, 384, v16\n"
    "v_lshl_add_u32 v8, v17, 4, v8\n"
    "s_mul_i32 s47, s53, 1280\n"
    "s_add_u32 s47, s47, 72384\n"
    "v_mul_u32_u24 v9, 160, v16\n"
    "v_lshl_add_u32 v9, v17, 3, v9\n"
    "v_add_u32 v9, s47, v9\n"
    "v_lshl_add_u32 v22, v18, 1, v20\n"
    "v_xor_b32 v23, s53, v22\n"
    "v_lshlrev_b32 v23, 5, v23\n"
    "v_lshl_add_u32 v23, v19, 3, v23\n"
    "v_lshl_add_u32 v23, v22, 10, v23\n"
    "v_lshl_add_u32 v10, v21, 8, v23\n"
    "v_mul_u32_u24 v11, 160, v17\n"
    "v_lshl_add_u32 v11, v16, 4, v11\n"
    "v_add_u32 v11, 77504, v11\n"
    "v_and_b32 v22, 1, v18\n"
    "v_lshlrev_b32 v22, 6, v22\n"
    "v_lshl_add_u32 v12, v16, 4, v22\n"
    "v_add_u32 v12, 59840, v12\n"
    "v_lshlrev_b32 v13, 4, v16\n"
    "v_add_u32 v13, 59968, v13\n"
    "v_lshlrev_b32 v14, 3, v20\n"
    "v_add_u32 v14, 59696, v14\n"
    "v_lshlrev_b32 v15, 13, v20\n"
    "v_lshl_add_u32 v15, v21, 16, v15\n"
    "v_lshl_add_u32 v15, v17, 3, v15\n"
    "s_mov_b32 s51, 0x05040100\n"
    "s_mov_b32 s52, 0x07060302\n"
    "v_mov_b64 v[96:97], 0\n"
    "v_mov_b64 v[98:99], 0\n"
    "v_mov_b64 v[100:101], 0\n"
    "v_mov_b64 v[102:103], 0\n"
    "v_mov_b64 v[104:105], 0\n"
    "v_mov_b64 v[106:107], 0\n"
    "v_mov_b64 v[108:109], 0\n"
    "v_mov_b64 v[110:111], 0\n"
    "v_mov_b32 v112, 0x3f803f80\n"
    "v_mov_b32 v113, 0x3f803f80\n"
    "v_mov_b32 v114, 0x3f803f80\n"
    "v_mov_b32 v115, 0x3f803f80\n"
    "s_lshl3_add_u32 s46, s29, s53\n"
    "s_lshl_b32 s46, s46, 5\n"
    "s_and_b32 s47, s30, 1\n"
    "s_lshl4_add_u32 s46, s47, s46\n"
    "s_lshr_b32 s47, s30, 1\n"
    "s_add_u32 s46, s46, s47\n"
    "s_mul_i32 s46, s46, 384\n"
    "s_add_u32 s46, s46, 528384\n"
    "s_add_u32 s38, s12, s46\n"
    "s_addc_u32 s39, s13, 0\n"
    "s_lshl2_add_u32 s46, s35, s53\n"
    "s_mul_i32 s46, s46, 3072\n"
    "s_add_u32 s46, s46, 516096\n"
    "s_add_u32 s40, s12, s46\n"
    "s_addc_u32 s41, s13, 0\n"
    "s_lshl3_add_u32 s46, s57, s53\n"
    "s_lshl_b32 s46, s46, 7\n"
    "s_add_u32 s46, s46, s35\n"
    "s_lshl_b32 s46, s46, 7\n"
    "s_add_u32 s46, s46, 1314816\n"
    "s_add_u32 s44, s12, s46\n"
    "s_addc_u32 s45, s13, 0\n"
    "s_mov_b32 s49, 2139095040\n"
    "s_mov_b32 s48, 1040356334\n"
    "v_mov_b32 v18, s53\n"
    "v_lshlrev_b32 v18, 2, v18\n"
    "s_mov_b32 s95, 0\n"
    "s_waitcnt vmcnt(3)\n"
    "global_load_dwordx4 v[48:51], v116, s[60:61] nt\n"
    "v_add_u16 v116, s50, v116\n"
    "s_waitcnt vmcnt(3)\n"
    "global_load_dwordx4 v[52:55], v116, s[60:61] nt\n"
    "v_add_u16 v116, s50, v116\n"
    "s_waitcnt vmcnt(3)\n"
    "global_load_dwordx4 v[56:59], v116, s[60:61] nt\n"
    "v_add_u16 v116, s50, v116\n"
    "s_waitcnt vmcnt(3)\n"
    "global_load_dwordx4 v[60:63], v116, s[60:61] nt\n"
    "s_waitcnt vmcnt(3)\n"
    "global_load_dwordx4 v[64:67], v117, s[62:63] nt\n"
    "v_add_u16 v117, s50, v117\n"
    "s_waitcnt vmcnt(3)\n"
    "global_load_dwordx4 v[68:71], v117, s[62:63] nt\n"
    "v_add_u16 v117, s50, v117\n"
    "s_waitcnt vmcnt(3)\n"
    "global_load_dwordx4 v[72:75], v117, s[62:63] nt\n"
    "v_add_u16 v117, s50, v117\n"
    "s_waitcnt vmcnt(3)\n"
    "global_load_dwordx4 v[76:79], v117, s[62:63] nt\n"
    "v_add_u16 v117, s50, v117\n"
    "ds_read_b128 v[20:23], v2 offset:58368\n"
    "s_mov_b32 s94, 1\n"
    "s_waitcnt lgkmcnt(0)\n"
    "v_max3_f32 v19, v20, v21, v22\n"
    "v_max_f32 v19, v19, v23\n"
    "v_cmp_eq_u32 vcc, s49, v19\n"
    "s_cbranch_vccz .Lfd_QK\n"
    ".Lfd_RET1:\n"
    "s_waitcnt vmcnt(3)\n"
    "global_load_dwordx4 v[80:83], v117, s[62:63] nt\n"
    "v_add_u16 v117, s50, v117\n"
    "s_waitcnt vmcnt(3)\n"
    "global_load_dwordx4 v[84:87], v117, s[62:63] nt\n"
    "v_add_u16 v117, s50, v117\n"
    "s_cmp_lg_u32 s95, 0\n"
    "s_cbranch_scc1 .Lfd_RET2\n"
    "ds_read_b128 v[20:23], v2 offset:58368\n"
    "s_mov_b32 s94, 2\n"
    "s_waitcnt lgkmcnt(0)\n"
    "v_max3_f32 v19, v20, v21, v22\n"
    "v_max_f32 v19, v19, v23\n"
    "v_cmp_eq_u32 vcc, s49, v19\n"
    "s_cbranch_vccz .Lfd_QK\n"
    ".Lfd_RET2:\n"
    "s_waitcnt vmcnt(3)\n"
    "global_load_dwordx4 v[88:91], v117, s[62:63] nt\n"
    "v_add_u16 v117, s50, v117\n"
    "s_waitcnt vmcnt(3)\n"
    "global_load_dwordx4 v[92:95], v117, s[62:63] nt\n"
    "s_mov_b64 exec, 1\n"
    "ds_write_b32 v18, v18 offset:59600\n"
    "s_mov_b64 exec, -1\n"
    "s_cmp_lg_u32 s95, 0\n"
    "s_cbranch_scc1 .Lfd_QKD\n"
    "s_mov_b32 s94, 0\n"
    "s_mov_b32 s37, 2097152\n"
    "s_branch .Lfd_G0\n"
    ".Lfd_R0:\n"
    "s_sub_u32 s37, s37, 1\n"
    "s_cbranch_scc1 .Lfd_D0\n"
    "s_sleep 1\n"
    ".Lfd_G0:\n"
    "ds_read_b128 v[20:23], v2 offset:58368\n"
    "s_waitcnt lgkmcnt(0)\n"
    "v_max3_f32 v19, v20, v21, v22\n"
    "v_max_f32 v19, v19, v23\n"
    "v_cmp_eq_u32 vcc, s49, v19\n"
    "s_cbranch_vccnz .Lfd_R0\n"
    ".Lfd_D0:\n"
    ".Lfd_QK:\n"
    "v_mfma_f32_16x16x32_bf16 v[32:35], v[32:35], v[20:23], 0\n"
    "v_mfma_f32_16x16x32_bf16 v[36:39], v[36:39], v[20:23], 0\n"
    "v_mfma_f32_16x16x32_bf16 v[40:43], v[40:43], v[20:23], 0\n"
    "v_mfma_f32_16x16x32_bf16 v[44:47], v[44:47], v[20:23], 0\n"
    "v_mfma_f32_16x16x32_bf16 v[48:51], v[48:51], v[20:23], 0\n"
    "v_mfma_f32_16x16x32_bf16 v[52:55], v[52:55], v[20:23], 0\n"
    "v_mfma_f32_16x16x32_bf16 v[56:59], v[56:59], v[20:23], 0\n"
    "v_mfma_f32_16x16x32_bf16 v[60:63], v[60:63], v[20:23], 0\n"
    "s_nop 0\n"
    "v_mov_b32_dpp v24, v32 quad_perm:[0,0,0,0] row_mask:0xf bank_mask:0xf\n"
    "v_mov_b32_dpp v25, v36 quad_perm:[0,0,0,0] row_mask:0xf bank_mask:0xf\n"
    "v_mov_b32_dpp v26, v40 quad_perm:[0,0,0,0] row_mask:0xf bank_mask:0xf\n"
    "v_mov_b32_dpp v27, v44 quad_perm:[0,0,0,0] row_mask:0xf bank_mask:0xf\n"
    "v_mov_b32_dpp v28, v48 quad_perm:[0,0,0,0] row_mask:0xf bank_mask:0xf\n"
    "v_mov_b32_dpp v29, v52 quad_perm:[0,0,0,0] row_mask:0xf bank_mask:0xf\n"
    "v_mov_b32_dpp v30, v56 quad_perm:[0,0,0,0] row_mask:0xf bank_mask:0xf\n"
    "v_mov_b32_dpp v31, v60 quad_perm:[0,0,0,0] row_mask:0xf bank_mask:0xf\n"
    "v_add_f32_dpp v24, v33, v24 quad_perm:[1,1,1,1] row_mask:0xf bank_mask:0xf\n"
    "v_add_f32_dpp v25, v37, v25 quad_perm:[1,1,1,1] row_mask:0xf bank_mask:0xf\n"
    "v_add_f32_dpp v26, v41, v26 quad_perm:[1,1,1,1] row_mask:0xf bank_mask:0xf\n"
    "v_add_f32_dpp v27, v45, v27 quad_perm:[1,1,1,1] row_mask:0xf bank_mask:0xf\n"
    "v_add_f32_dpp v28, v49, v28 quad_perm:[1,1,1,1] row_mask:0xf bank_mask:0xf\n"
    "v_add_f32_dpp v29, v53, v29 quad_perm:[1,1,1,1] row_mask:0xf bank_mask:0xf\n"
    "v_add_f32_dpp v30, v57, v30 quad_perm:[1,1,1,1] row_mask:0xf bank_mask:0xf\n"
    "v_add_f32_dpp v31, v61, v31 quad_perm:[1,1,1,1] row_mask:0xf bank_mask:0xf\n"
    "v_add_f32_dpp v24, v34, v24 quad_perm:[2,2,2,2] row_mask:0xf bank_mask:0xf\n"
    "v_add_f32_dpp v25, v38, v25 quad_perm:[2,2,2,2] row_mask:0xf bank_mask:0xf\n"
    "v_add_f32_dpp v26, v42, v26 quad_perm:[2,2,2,2] row_mask:0xf bank_mask:0xf\n"
    "v_add_f32_dpp v27, v46, v27 quad_perm:[2,2,2,2] row_mask:0xf bank_mask:0xf\n"
    "v_add_f32_dpp v28, v50, v28 quad_perm:[2,2,2,2] row_mask:0xf bank_mask:0xf\n"
    "v_add_f32_dpp v29, v54, v29 quad_perm:[2,2,2,2] row_mask:0xf bank_mask:0xf\n"
    "v_add_f32_dpp v30, v58, v30 quad_perm:[2,2,2,2] row_mask:0xf bank_mask:0xf\n"
    "v_add_f32_dpp v31, v62, v31 quad_perm:[2,2,2,2] row_mask:0xf bank_mask:0xf\n"
    "v_add_f32_dpp v24, v35, v24 quad_perm:[3,3,3,3] row_mask:0xf bank_mask:0xf\n"
    "v_add_f32_dpp v25, v39, v25 quad_perm:[3,3,3,3] row_mask:0xf bank_mask:0xf\n"
    "v_add_f32_dpp v26, v43, v26 quad_perm:[3,3,3,3] row_mask:0xf bank_mask:0xf\n"
    "v_add_f32_dpp v27, v47, v27 quad_perm:[3,3,3,3] row_mask:0xf bank_mask:0xf\n"
    "v_add_f32_dpp v28, v51, v28 quad_perm:[3,3,3,3] row_mask:0xf bank_mask:0xf\n"
    "v_add_f32_dpp v29, v55, v29 quad_perm:[3,3,3,3] row_mask:0xf bank_mask:0xf\n"
    "v_add_f32_dpp v30, v59, v30 quad_perm:[3,3,3,3] row_mask:0xf bank_mask:0xf\n"
    "v_add_f32_dpp v31, v63, v31 quad_perm:[3,3,3,3] row_mask:0xf bank_mask:0xf\n"
    "v_max3_f32 v119, v24, v25, v26\n"
    "v_max3_f32 v19, v27, v28, v29\n"
    "v_max3_f32 v119, v119, v30, v31\n"
    "v_max_f32 v119, v119, v19\n"
    "v_mov_b32 v19, v119\n"
    "s_nop 1\n"
    "v_permlane16_swap_b32 v119, v19\n"
    "v_max_f32 v119, v119, v19\n"
    "v_mov_b32 v19, v119\n"
    "s_nop 1\n"
    "v_permlane32_swap_b32 v119, v19\n"
    "v_max_f32 v119, v119, v19\n"
    "s_mov_b64 exec, 0x1111\n"
    "ds_write_b32 v4, v119\n"
    "s_mov_b64 exec, -1\n"
    "s_mov_b32 s95, 1\n"
    "s_cmp_eq_u32 s94, 1\n"
    "s_cbranch_scc1 .Lfd_RET1\n"
    "s_cmp_eq_u32 s94, 2\n"
    "s_cbranch_scc1 .Lfd_RET2\n"
    ".Lfd_QKD:\n"
    "s_mul_i32 s36, s56, 1664525\n"
    "s_add_u32 s36, s36, 1013904223\n"
    "s_mov_b32 s37, 2097152\n"
    "s_branch .Lfd_G1\n"
    ".Lfd_R1:\n"
    "s_sub_u32 s37, s37, 1\n"
    "s_cbranch_scc1 .Lfd_D1\n"
    ".Lfd_G1:\n"
    "ds_read_b128 v[32:35], v3 offset:59456\n"
    "ds_read_b128 v[36:39], v3 offset:59472\n"
    "s_waitcnt lgkmcnt(0)\n"
    "v_max3_f32 v119, v32, v33, v34\n"
    "v_max3_f32 v19, v35, v36, v37\n"
    "v_max3_f32 v119, v119, v38, v39\n"
    "v_max_f32 v119, v119, v19\n"
    "v_cmp_eq_u32 vcc, s49, v119\n"
    "s_cbranch_vccnz .Lfd_R1\n"
    ".Lfd_D1:\n"
    "v_mul_f32 v119, s48, v119\n"
    "v_fma_f32 v24, v24, s48, -v119\n"
    "v_fma_f32 v25, v25, s48, -v119\n"
    "v_fma_f32 v26, v26, s48, -v119\n"
    "v_fma_f32 v27, v27, s48, -v119\n"
    "v_fma_f32 v28, v28, s48, -v119\n"
    "v_fma_f32 v29, v29, s48, -v119\n"
    "v_fma_f32 v30, v30, s48, -v119\n"
    "v_fma_f32 v31, v31, s48, -v119\n"
    "v_exp_f32 v24, v24\n"
    "v_exp_f32 v25, v25\n"
    "v_exp_f32 v26, v26\n"
    "v_exp_f32 v27, v27\n"
    "v_exp_f32 v28, v28\n"
    "v_exp_f32 v29, v29\n"
    "v_exp_f32 v30, v30\n"
    "v_exp_f32 v31, v31\n"
    "s_mov_b32 exec_lo, 0x11111111\n"
    "s_mov_b32 exec_hi, 0x11111111\n"
    "v_cvt_pk_bf16_f32 v96, v24, v25\n"
    "v_cvt_pk_bf16_f32 v100, v26, v27\n"
    "v_cvt_pk_bf16_f32 v104, v28, v29\n"
    "v_cvt_pk_bf16_f32 v108, v30, v31\n"
    "s_mov_b32 exec_lo, 0x22222222\n"
    "s_mov_b32 exec_hi, 0x22222222\n"
    "v_cvt_pk_bf16_f32 v97, v24, v25\n"
    "v_cvt_pk_bf16_f32 v101, v26, v27\n"
    "v_cvt_pk_bf16_f32 v105, v28, v29\n"
    "v_cvt_pk_bf16_f32 v109, v30, v31\n"
    "s_mov_b32 exec_lo, 0x44444444\n"
    "s_mov_b32 exec_hi, 0x44444444\n"
    "v_cvt_pk_bf16_f32 v98, v24, v25\n"
    "v_cvt_pk_bf16_f32 v102, v26, v27\n"
    "v_cvt_pk_bf16_f32 v106, v28, v29\n"
    "v_cvt_pk_bf16_f32 v110, v30, v31\n"
    "s_mov_b32 exec_lo, 0x88888888\n"
    "s_mov_b32 exec_hi, 0x88888888\n"
    "v_cvt_pk_bf16_f32 v99, v24, v25\n"
    "v_cvt_pk_bf16_f32 v103, v26, v27\n"
    "v_cvt_pk_bf16_f32 v107, v28, v29\n"
    "v_cvt_pk_bf16_f32 v111, v30, v31\n"
    "s_mov_b64 exec, -1\n"
    "s_nop 0\n"
    "v_mfma_f32_16x16x32_bf16 v[20:23], v[112:115], v[96:99], 0\n"
    "v_mfma_f32_16x16x32_bf16 v[20:23], v[112:115], v[100:103], v[20:23]\n"
    "v_mfma_f32_16x16x32_bf16 v[20:23], v[112:115], v[104:107], v[20:23]\n"
    "v_mfma_f32_16x16x32_bf16 v[20:23], v[112:115], v[108:111], v[20:23]\n"
    "v_readlane_b32 s46, v119, s59\n"
    "v_mov_b32 v29, s36\n"
    "v_mov_b32 v31, s36\n"
    "v_mov_b32 v19, s58\n"
    "s_nop 3\n"
    "s_mov_b64 exec, 0x1111\n"
    "ds_write_b32 v4, v20 offset:256\n"
    "s_mov_b64 exec, -1\n"
    "s_waitcnt vmcnt(6)\n"
    "v_perm_b32 v32, v68, v64, s51\n"
    "v_perm_b32 v33, v69, v65, s51\n"
    "v_perm_b32 v34, v70, v66, s51\n"
    "v_perm_b32 v35, v71, v67, s51\n"
    "v_perm_b32 v36, v68, v64, s52\n"
    "v_perm_b32 v37, v69, v65, s52\n"
    "v_perm_b32 v38, v70, v66, s52\n"
    "v_perm_b32 v39, v71, v67, s52\n"
    "v_mfma_f32_16x16x32_bf16 v[20:23], v[32:35], v[96:99], 0\n"
    "s_nop 0\n"
    "v_mfma_f32_16x16x32_bf16 v[24:27], v[36:39], v[96:99], 0\n"
    "s_waitcnt vmcnt(4)\n"
    "v_perm_b32 v32, v76, v72, s51\n"
    "v_perm_b32 v33, v77, v73, s51\n"
    "v_perm_b32 v34, v78, v74, s51\n"
    "v_perm_b32 v35, v79, v75, s51\n"
    "v_perm_b32 v36, v76, v72, s52\n"
    "v_perm_b32 v37, v77, v73, s52\n"
    "v_perm_b32 v38, v78, v74, s52\n"
    "v_perm_b32 v39, v79, v75, s52\n"
    "v_mfma_f32_16x16x32_bf16 v[20:23], v[32:35], v[100:103], v[20:23]\n"
    "s_nop 0\n"
    "v_mfma_f32_16x16x32_bf16 v[24:27], v[36:39], v[100:103], v[24:27]\n"
    "s_waitcnt vmcnt(2)\n"
    "v_perm_b32 v32, v84, v80, s51\n"
    "v_perm_b32 v33, v85, v81, s51\n"
    "v_perm_b32 v34, v86, v82, s51\n"
    "v_perm_b32 v35, v87, v83, s51\n"
    "v_perm_b32 v36, v84, v80, s52\n"
    "v_perm_b32 v37, v85, v81, s52\n"
    "v_perm_b32 v38, v86, v82, s52\n"
    "v_perm_b32 v39, v87, v83, s52\n"
    "v_mfma_f32_16x16x32_bf16 v[20:23], v[32:35], v[104:107], v[20:23]\n"
    "s_nop 0\n"
    "v_mfma_f32_16x16x32_bf16 v[24:27], v[36:39], v[104:107], v[24:27]\n"
    "s_mov_b32 s37, 2097152\n"
    "s_branch .Lfd_G2\n"
    ".Lfd_R2:\n"
    "s_sub_u32 s37, s37, 1\n"
    "s_cbranch_scc1 .Lfd_D2\n"
    ".Lfd_G2:\n"
    "ds_read_b128 v[40:43], v19\n"
    "ds_read_b128 v[44:47], v19 offset:16\n"
    "s_waitcnt lgkmcnt(0)\n"
    "v_pk_add_f32 v[40:41], v[40:41], v[42:43]\n"
    "v_pk_add_f32 v[44:45], v[44:45], v[46:47]\n"
    "v_pk_add_f32 v[40:41], v[40:41], v[44:45]\n"
    "v_add_f32 v118, v40, v41\n"
    "v_cmp_eq_u32 vcc, s49, v118\n"
    "s_cbranch_vccnz .Lfd_R2\n"
    ".Lfd_D2:\n"
    "v_mov_b32 v28, s46\n"
    "v_mov_b32 v30, v118\n"
    "s_mov_b32 s58, 0x55555555\n"
    "s_mov_b32 s59, 0x55555555\n"
    "s_mov_b32 s42, 0x33333333\n"
    "s_mov_b32 s43, 0x11111111\n"
    "s_waitcnt vmcnt(0)\n"
    "v_perm_b32 v32, v92, v88, s51\n"
    "v_perm_b32 v33, v93, v89, s51\n"
    "v_perm_b32 v34, v94, v90, s51\n"
    "v_perm_b32 v35, v95, v91, s51\n"
    "v_perm_b32 v36, v92, v88, s52\n"
    "v_perm_b32 v37, v93, v89, s52\n"
    "v_perm_b32 v38, v94, v90, s52\n"
    "v_perm_b32 v39, v95, v91, s52\n"
    "v_mfma_f32_16x16x32_bf16 v[20:23], v[32:35], v[108:111], v[20:23]\n"
    "s_nop 0\n"
    "v_mfma_f32_16x16x32_bf16 v[24:27], v[36:39], v[108:111], v[24:27]\n"
    "s_nop 5\n"
    "ds_write_b128 v5, v[20:23]\n"
    "s_nop 0\n"
    "ds_write_b128 v5, v[24:27] offset:1088\n"
    "ds_read_b32 v32, v6 offset:0\n"
    "ds_read_b32 v33, v6 offset:2176\n"
    "ds_read_b32 v34, v6 offset:4352\n"
    "ds_read_b32 v35, v6 offset:6528\n"
    "ds_read_b32 v36, v6 offset:8704\n"
    "ds_read_b32 v37, v6 offset:10880\n"
    "ds_read_b32 v38, v6 offset:13056\n"
    "ds_read_b32 v39, v6 offset:15232\n"
    "s_mov_b32 s37, 2097152\n"
    "s_waitcnt lgkmcnt(4)\n"
    ".Lfd_MA:\n"
    "v_pk_add_f32 v[40:41], v[32:33], v[34:35]\n"
    "v_add_f32 v40, v40, v41\n"
    "s_waitcnt lgkmcnt(0)\n"
    ".Lfd_MB:\n"
    "v_pk_add_f32 v[42:43], v[36:37], v[38:39]\n"
    "v_add_f32 v42, v42, v43\n"
    "v_add_f32 v44, v40, v42\n"
    "v_cmp_eq_u32 vcc, s49, v44\n"
    "s_nop 0\n"
    "v_mov_b32_dpp v45, v44 quad_perm:[1,1,3,3] row_mask:0xf bank_mask:0xf\n"
    "s_mov_b64 exec, s[58:59]\n"
    "v_cvt_pk_bf16_f32 v28, v44, v45\n"
    "s_nop 1\n"
    "v_mov_b32_dpp v30, v28 quad_perm:[2,2,2,2] row_mask:0xf bank_mask:0xf\n"
    "s_cbranch_vccnz .Lfd_MR\n"
    ".Lfd_MD:\n"
    "s_mov_b64 exec, s[42:43]\n"
    "global_store_dwordx4 v7, v[28:31], s[38:39] sc1\n"
    "s_mov_b64 exec, -1\n"
    "s_cmp_lt_u32 s53, 4\n"
    "s_cbranch_scc1 .Lfd_FINAL\n"
    "s_sleep 4\n"
    "v_mov_b32 v16, 0\n"
    "s_mov_b32 s37, 2097152\n"
    "s_branch .Lfd_G3\n"
    ".Lfd_R3:\n"
    "s_sub_u32 s37, s37, 1\n"
    "s_cbranch_scc1 .Lfd_D3\n"
    "s_sleep 1\n"
    ".Lfd_G3:\n"
    "ds_read_b128 v[32:35], v16 offset:59680\n"
    "s_waitcnt lgkmcnt(0)\n"
    "v_max3_u32 v32, v32, v33, v34\n"
    "v_max_u32 v32, v32, v35\n"
    "v_cmp_eq_u32 vcc, s49, v32\n"
    "s_cbranch_vccnz .Lfd_R3\n"
    ".Lfd_D3:\n"
    "s_mov_b64 s[42:43], exec\n"
    "s_mov_b32 s37, 131072\n"
    "global_load_dwordx4 v[32:35], v8, s[40:41] sc1\n"
    "global_load_dwordx4 v[36:39], v8, s[40:41] offset:1536 sc1\n"
    "s_sleep 7\n"
    ".Lfd_G4:\n"
    "global_load_dwordx4 v[40:43], v8, s[40:41] sc1\n"
    "global_load_dwordx4 v[44:47], v8, s[40:41] offset:1536 sc1\n"
    "s_waitcnt vmcnt(2)\n"
    "v_bitop3_b32 v33, v33, v35, s36 bitop3:0x7e\n"
    "v_bitop3_b32 v37, v37, v39, s36 bitop3:0x7e\n"
    "v_or_b32 v33, v33, v37\n"
    "v_cmpx_eq_u32 vcc, 0, v33\n"
    "v_mov_b32 v33, v34\n"
    "v_mov_b32 v37, v38\n"
    "ds_write_b64 v9, v[32:33]\n"
    "ds_write_b64 v9, v[36:37] offset:640\n"
    "s_andn2_b64 exec, s[42:43], exec\n"
    "s_mov_b64 s[42:43], exec\n"
    "s_cbranch_execz .Lfd_D4\n"
    "global_load_dwordx4 v[32:35], v8, s[40:41] sc1\n"
    "global_load_dwordx4 v[36:39], v8, s[40:41] offset:1536 sc1\n"
    "s_waitcnt vmcnt(2)\n"
    "v_bitop3_b32 v41, v41, v43, s36 bitop3:0x7e\n"
    "v_bitop3_b32 v45, v45, v47, s36 bitop3:0x7e\n"
    "v_or_b32 v41, v41, v45\n"
    "v_cmpx_eq_u32 vcc, 0, v41\n"
    "v_mov_b32 v41, v42\n"
    "v_mov_b32 v45, v46\n"
    "ds_write_b64 v9, v[40:41]\n"
    "ds_write_b64 v9, v[44:45] offset:640\n"
    "s_andn2_b64 exec, s[42:43], exec\n"
    "s_mov_b64 s[42:43], exec\n"
    "s_cbranch_execz .Lfd_D4\n"
    "s_sub_u32 s37, s37, 1\n"
    "s_cbranch_scc0 .Lfd_G4\n"
    ".Lfd_D4:\n"
    "s_mov_b64 exec, -1\n"
    "s_endpgm\n"
    ".Lfd_FINAL:\n"
    "v_mov_b32 v16, 0\n"
    "v_xor_b32 v17, 128, v10\n"
    "s_mov_b32 s58, 0\n"
    "s_mov_b32 s59, -1\n"
    "s_mov_b32 s37, 2097152\n"
    "s_branch .Lfd_G5\n"
    ".Lfd_R5:\n"
    "s_sub_u32 s37, s37, 1\n"
    "s_cbranch_scc1 .Lfd_D5\n"
    "s_sleep 1\n"
    ".Lfd_G5:\n"
    "ds_read_b128 v[32:35], v16 offset:59632\n"
    "ds_read_b32 v84, v14 offset:4\n"
    "s_waitcnt lgkmcnt(0)\n"
    "v_max3_u32 v32, v32, v33, v34\n"
    "v_max3_u32 v32, v32, v35, v84\n"
    "v_cmp_eq_u32 vcc, s49, v32\n"
    "s_cbranch_vccnz .Lfd_R5\n"
    ".Lfd_D5:\n"
    "ds_read_b64_tr_b16 v[32:33], v10 offset:32768\n"
    "ds_read_b64_tr_b16 v[34:35], v10 offset:33280\n"
    "ds_read_b64_tr_b16 v[36:37], v10 offset:40960\n"
    "ds_read_b64_tr_b16 v[38:39], v10 offset:41472\n"
    "ds_read_b64_tr_b16 v[40:41], v10 offset:49152\n"
    "ds_read_b64_tr_b16 v[42:43], v10 offset:49664\n"
    "ds_read_b64_tr_b16 v[44:45], v17 offset:32768\n"
    "ds_read_b64_tr_b16 v[46:47], v17 offset:33280\n"
    "ds_read_b64_tr_b16 v[48:49], v17 offset:40960\n"
    "ds_read_b64_tr_b16 v[50:51], v17 offset:41472\n"
    "ds_read_b64_tr_b16 v[52:53], v17 offset:49152\n"
    "ds_read_b64_tr_b16 v[54:55], v17 offset:49664\n"
    "s_mov_b32 s37, 2097152\n"
    "s_waitcnt lgkmcnt(8)\n"
    "s_branch .Lfd_G6\n"
    ".Lfd_R6:\n"
    "s_sub_u32 s37, s37, 1\n"
    "s_cbranch_scc1 .Lfd_D6\n"
    "s_sleep 1\n"
    ".Lfd_G6:\n"
    "ds_read_b128 v[56:59], v11\n"
    "ds_read_b128 v[60:63], v11 offset:2560\n"
    "ds_read_b128 v[64:67], v11 offset:64\n"
    "ds_read_b128 v[68:71], v11 offset:2624\n"
    "ds_read_b128 v[76:79], v13\n"
    "ds_read_b128 v[80:83], v13 offset:64\n"
    "ds_read_b128 v[72:75], v12\n"
    "s_waitcnt lgkmcnt(5)\n"
    "v_max3_f32 v16, v56, v58, v60\n"
    "s_waitcnt lgkmcnt(3)\n"
    "v_max3_f32 v17, v62, v64, v66\n"
    "v_max3_f32 v16, v16, v68, v70\n"
    "v_max_f32 v16, v16, v17\n"
    "v_cmp_eq_u32 vcc, s49, v16\n"
    "v_mfma_f32_16x16x32_bf16 v[20:23], v[56:59], v[32:35], 0\n"
    "v_mfma_f32_16x16x32_bf16 v[24:27], v[60:63], v[32:35], 0\n"
    "s_cbranch_vccnz .Lfd_R6\n"
    ".Lfd_D6:\n"
    "v_mfma_f32_16x16x32_bf16 v[20:23], v[64:67], v[36:39], v[20:23]\n"
    "v_mfma_f32_16x16x32_bf16 v[24:27], v[68:71], v[36:39], v[24:27]\n"
    "s_waitcnt lgkmcnt(0)\n"
    "v_max3_f32 v16, v76, v77, v78\n"
    "v_max3_f32 v85, v79, v80, v81\n"
    "v_mfma_f32_16x16x32_bf16 v[88:91], v[56:59], v[44:47], 0\n"
    "v_max3_f32 v16, v16, v82, v83\n"
    "v_max3_f32 v17, v72, v73, v74\n"
    "v_max_f32 v16, v16, v85\n"
    "v_mfma_f32_16x16x32_bf16 v[92:95], v[60:63], v[44:47], 0\n"
    "v_max_f32 v17, v17, v75\n"
    "v_cmp_eq_u32 vcc, s49, v16\n"
    "v_cmp_eq_u32 s[42:43], s49, v17\n"
    "v_mfma_f32_16x16x32_bf16 v[88:91], v[64:67], v[48:51], v[88:91]\n"
    "s_mov_b32 s37, 2097152\n"
    "v_mfma_f32_16x16x32_bf16 v[92:95], v[68:71], v[48:51], v[92:95]\n"
    "s_cbranch_vccnz .Lfd_WR\n"
    ".Lfd_WD:\n"
    "s_cmp_lg_u64 s[42:43], 0\n"
    "s_cbranch_scc1 .Lfd_NOH\n"
    "v_mfma_f32_16x16x32_bf16 v[96:99], v[72:75], v[40:43], 0\n"
    "v_mfma_f32_16x16x32_bf16 v[100:103], v[72:75], v[52:55], 0\n"
    ".Lfd_NOH:\n"
    "v_pk_mul_f32 v[20:21], v[20:21], v[76:77]\n"
    "v_pk_mul_f32 v[24:25], v[24:25], v[80:81]\n"
    "v_pk_fma_f32 v[20:21], v[22:23], v[78:79], v[20:21]\n"
    "v_pk_fma_f32 v[24:25], v[26:27], v[82:83], v[24:25]\n"
    "v_add_f32 v20, v20, v21\n"
    "v_add_f32 v24, v24, v25\n"
    "v_pk_mul_f32 v[88:89], v[88:89], v[76:77]\n"
    "v_pk_mul_f32 v[92:93], v[92:93], v[80:81]\n"
    "v_pk_fma_f32 v[88:89], v[90:91], v[78:79], v[88:89]\n"
    "v_pk_fma_f32 v[92:93], v[94:95], v[82:83], v[92:93]\n"
    "v_permlane16_swap_b32 v20, v24\n"
    "v_add_f32 v88, v88, v89\n"
    "v_add_f32 v92, v92, v93\n"
    "v_add_f32 v20, v20, v24\n"
    "s_nop 0\n"
    "v_permlane16_swap_b32 v88, v92\n"
    "v_add_f32 v88, v88, v92\n"
    "s_nop 1\n"
    "v_permlane32_swap_b32 v20, v88\n"
    "v_add_f32 v28, v20, v88\n"
    "s_cbranch_scc1 .Lfd_HR\n"
    ".Lfd_HD:\n"
    "v_cndmask_b32 v96, v96, v100, s[58:59]\n"
    "v_fmac_f32 v28, v84, v96\n"
    "s_nop 1\n"
    "v_mov_b32_dpp v30, v28 quad_perm:[1,0,3,2] row_mask:0xf bank_mask:0xf\n"
    "s_mov_b32 exec_lo, 0x55555555\n"
    "s_mov_b32 exec_hi, 0x55555555\n"
    "global_store_dwordx4 v15, v[28:31], s[44:45] sc1\n"
    "s_mov_b64 exec, 1\n"
    "ds_write_b32 v18, v18 offset:59664\n"
    "s_endpgm\n"
    ".Lfd_MR:\n"
    "s_mov_b64 exec, -1\n"
    "s_sub_u32 s37, s37, 1\n"
    "s_cbranch_scc1 .Lfd_MD\n"
    "v_cmp_eq_u32 vcc, s49, v40\n"
    "s_cbranch_vccnz .Lfd_MRA\n"
    "ds_read_b32 v36, v6 offset:8704\n"
    "ds_read_b32 v37, v6 offset:10880\n"
    "ds_read_b32 v38, v6 offset:13056\n"
    "ds_read_b32 v39, v6 offset:15232\n"
    "s_waitcnt lgkmcnt(0)\n"
    "s_branch .Lfd_MB\n"
    ".Lfd_MRA:\n"
    "s_sleep 1\n"
    "ds_read_b32 v32, v6 offset:0\n"
    "ds_read_b32 v33, v6 offset:2176\n"
    "ds_read_b32 v34, v6 offset:4352\n"
    "ds_read_b32 v35, v6 offset:6528\n"
    "ds_read_b32 v36, v6 offset:8704\n"
    "ds_read_b32 v37, v6 offset:10880\n"
    "ds_read_b32 v38, v6 offset:13056\n"
    "ds_read_b32 v39, v6 offset:15232\n"
    "s_waitcnt lgkmcnt(0)\n"
    "s_branch .Lfd_MA\n"
    ".Lfd_HR:\n"
    "ds_read_b128 v[72:75], v12\n"
    "s_sub_u32 s37, s37, 1\n"
    "s_cbranch_scc1 .Lfd_HG\n"
    "s_waitcnt lgkmcnt(0)\n"
    "v_max3_f32 v17, v72, v73, v74\n"
    "v_max_f32 v17, v17, v75\n"
    "v_cmp_eq_u32 vcc, s49, v17\n"
    "s_cbranch_vccnz .Lfd_HR\n"
    ".Lfd_HG:\n"
    "s_waitcnt lgkmcnt(0)\n"
    "v_mfma_f32_16x16x32_bf16 v[96:99], v[72:75], v[40:43], 0\n"
    "v_mfma_f32_16x16x32_bf16 v[100:103], v[72:75], v[52:55], 0\n"
    "s_nop 6\n"
    "s_branch .Lfd_HD\n"
    ".Lfd_WR:\n"
    "ds_read_b128 v[72:75], v12\n"
    "ds_read_b128 v[76:79], v13\n"
    "ds_read_b128 v[80:83], v13 offset:64\n"
    "s_sub_u32 s37, s37, 1\n"
    "s_waitcnt lgkmcnt(0)\n"
    "s_cbranch_scc1 .Lfd_WD\n"
    "v_max3_f32 v16, v76, v77, v78\n"
    "v_max3_f32 v85, v79, v80, v81\n"
    "v_max3_f32 v16, v16, v82, v83\n"
    "v_max_f32 v16, v16, v85\n"
    "v_max3_f32 v17, v72, v73, v74\n"
    "v_max_f32 v17, v17, v75\n"
    "v_cmp_eq_u32 s[42:43], s49, v17\n"
    "v_cmp_eq_u32 vcc, s49, v16\n"
    "s_cbranch_vccnz .Lfd_WR\n"
    "s_branch .Lfd_WD\n"
    ".Lfd_KVTAIL:\n"
    ".p2align 6\n"
    ".Lfd_SMAIN:\n"
    "s_load_dword s56, s[12:13], 0\n"
    "s_and_b32 s57, s29, 3\n"
    "s_andn2_b32 s35, 4, s29\n"
    "s_lshl3_add_u32 s35, s35, s30\n"
    "v_lshrrev_b32 v6, 4, v1\n"
    "v_and_b32 v7, 15, v1\n"
    "v_lshrrev_b32 v28, 2, v7\n"
    "v_and_b32 v29, 3, v1\n"
    "v_lshrrev_b32 v30, 1, v28\n"
    "v_and_b32 v31, 1, v6\n"
    "v_lshl_or_b32 v30, v31, 1, v30\n"
    "v_xor_b32 v30, s28, v30\n"
    "v_lshl_add_u32 v31, v6, 3, v28\n"
    "v_lshlrev_b32 v3, 7, v31\n"
    "v_lshl_add_u32 v3, v30, 5, v3\n"
    "v_lshl_add_u32 v3, v29, 3, v3\n"
    "v_and_b32 v28, 1, v1\n"
    "v_lshlrev_b32 v4, 8, v28\n"
    "v_lshl_add_u32 v4, v6, 4, v4\n"
    "v_bfe_u32 v29, v1, 1, 1\n"
    "v_lshl_add_u32 v29, v6, 1, v29\n"
    "v_lshlrev_b32 v29, 4, v29\n"
    "v_lshlrev_b32 v5, 4, v6\n"
    "v_lshl_add_u32 v5, v28, 13, v5\n"
    "v_lshl_add_u32 v30, v28, 8, v29\n"
    "s_lshl_b32 s46, s46, 14\n"
    "s_lshr_b32 s47, s34, 1\n"
    "s_lshl2_add_u32 s47, s47, s31\n"
    "s_lshl_b32 s47, s47, 9\n"
    "s_add_u32 s46, s46, s47\n"
    "s_and_b32 s47, s34, 1\n"
    "s_lshl2_add_u32 s47, s47, s28\n"
    "s_lshl_b32 s47, s47, 6\n"
    "s_add_u32 s46, s46, s47\n"
    "s_add_u32 s46, s46, 4096\n"
    "s_add_u32 s38, s12, s46\n"
    "s_addc_u32 s39, s13, 0\n"
    "s_and_b32 s46, s30, 1\n"
    "s_lshl1_add_u32 s46, s29, s46\n"
    "s_lshl2_add_u32 s46, s46, s28\n"
    "s_lshl_b32 s46, s46, 11\n"
    "s_add_u32 s46, s46, 4096\n"
    "s_add_u32 s40, s12, s46\n"
    "s_addc_u32 s41, s13, 0\n"
    "s_lshr_b32 s46, s30, 3\n"
    "s_lshl2_add_u32 s54, s46, s57\n"
    "s_and_b32 s46, s29, 4\n"
    "s_lshl2_add_u32 s54, s46, s54\n"
    "s_mov_b32 s49, 2139095040\n"
    "s_mov_b32 s58, 0xcccccccc\n"
    "s_mov_b32 s59, 0xcccccccc\n"
    "s_lshl_b32 s46, s28, 2\n"
    "v_mov_b32 v28, s46\n"
    "v_mov_b32 v31, 0\n"
    "s_waitcnt vmcnt(0) lgkmcnt(0)\n"
    "s_mov_b64 exec, 1\n"
    "ds_write_b32 v28, v28 offset:59584\n"
    "s_mov_b64 exec, -1\n"
    "s_mul_i32 s36, s56, 1664525\n"
    "s_add_u32 s36, s36, 1013904223\n"
    "v_mov_b32 v25, s36\n"
    "v_mov_b32 v27, s36\n"
    "s_mov_b32 s37, 2097152\n"
    "s_branch .Lfd_G7\n"
    ".Lfd_R7:\n"
    "s_sub_u32 s37, s37, 1\n"
    "s_cbranch_scc1 .Lfd_D7\n"
    ".Lfd_G7:\n"
    "ds_read_b128 v[32:35], v31 offset:59584\n"
    "s_waitcnt lgkmcnt(0)\n"
    "v_max3_u32 v32, v32, v33, v34\n"
    "v_max_u32 v32, v32, v35\n"
    "v_cmp_eq_u32 vcc, s49, v32\n"
    "s_cbranch_vccnz .Lfd_R7\n"
    ".Lfd_D7:\n"
    "ds_read_b64_tr_b16 v[32:33], v3 offset:0\n"
    "ds_read_b64_tr_b16 v[34:35], v3 offset:512\n"
    "ds_read_b128 v[8:11], v4 offset:57344\n"
    "ds_read_b64_tr_b16 v[36:37], v3 offset:4096\n"
    "ds_read_b64_tr_b16 v[38:39], v3 offset:4608\n"
    "ds_read_b128 v[12:15], v4 offset:57408\n"
    "ds_read_b64_tr_b16 v[40:41], v3 offset:8192\n"
    "ds_read_b64_tr_b16 v[42:43], v3 offset:8704\n"
    "ds_read_b128 v[16:19], v4 offset:57472\n"
    "ds_read_b64_tr_b16 v[44:45], v3 offset:12288\n"
    "ds_read_b64_tr_b16 v[46:47], v3 offset:12800\n"
    "ds_read_b128 v[20:23], v4 offset:57536\n"
    "s_waitcnt lgkmcnt(9)\n"
    "v_mfma_f32_16x16x32_bf16 v[48:51], v[32:35], v[8:11], 0\n"
    "s_waitcnt lgkmcnt(6)\n"
    "v_mfma_f32_16x16x32_bf16 v[48:51], v[36:39], v[12:15], v[48:51]\n"
    "s_waitcnt lgkmcnt(3)\n"
    "v_mfma_f32_16x16x32_bf16 v[48:51], v[40:43], v[16:19], v[48:51]\n"
    "s_waitcnt lgkmcnt(0)\n"
    "v_mfma_f32_16x16x32_bf16 v[48:51], v[44:47], v[20:23], v[48:51]\n"
    "s_nop 7\n"
    "v_cvt_pk_bf16_f32 v24, v48, v49\n"
    "v_cvt_pk_bf16_f32 v26, v50, v51\n"
    "s_mov_b32 exec_lo, 0x00030003\n"
    "s_mov_b32 exec_hi, 0x00030003\n"
    "global_store_dwordx4 v5, v[24:27], s[38:39] sc1\n"
    "s_mov_b64 exec, -1\n"
    "v_lshlrev_b32 v29, 4, v1\n"
    "v_lshrrev_b32 v52, 3, v1\n"
    "v_mul_u32_u24 v52, 3584, v52\n"
    "v_add_u32 v52, v52, v2\n"
    "v_xor_b32 v53, 64, v52\n"
    "s_bitcmp1_b32 s30, 2\n"
    "s_cselect_b32 s90, s16, s14\n"
    "s_cselect_b32 s91, s17, s15\n"
    "s_lshl2_add_u32 s46, s31, s28\n"
    "s_mul_i32 s46, s46, 114688\n"
    "s_lshl_b32 s47, s54, 7\n"
    "s_add_u32 s46, s46, s47\n"
    "s_add_u32 s90, s90, s46\n"
    "s_addc_u32 s91, s91, 0\n"
    "v_mov_b32 v78, v28\n"
    "s_lshr_b32 s46, s28, 1\n"
    "s_lshl1_add_u32 s46, s54, s46\n"
    "s_lshl2_add_u32 s46, s46, s31\n"
    "s_and_b32 s47, s34, 1\n"
    "s_lshl1_add_u32 s46, s46, s47\n"
    "s_lshl_b32 s46, s46, 2\n"
    "s_and_b32 s48, s28, 1\n"
    "s_add_u32 s46, s46, s48\n"
    "s_lshl_b32 s46, s46, 7\n"
    "s_add_u32 s46, s46, 266240\n"
    "s_add_u32 s38, s12, s46\n"
    "s_addc_u32 s39, s13, 0\n"
    "s_sleep 34\n"
    "s_cmp_lg_u32 s28, 3\n"
    "s_cbranch_scc1 .Lfd_NOGL\n"
    "global_load_dwordx4 v[64:67], v29, s[4:5]\n"
    "global_load_dwordx4 v[68:71], v29, s[4:5] offset:1024\n"
    "global_load_dwordx4 v[72:75], v29, s[18:19]\n"
    ".Lfd_NOGL:\n"
    "s_mov_b64 s[42:43], exec\n"
    "s_mov_b32 s37, 131072\n"
    "s_branch .Lfd_G8\n"
    ".Lfd_R8:\n"
    "s_sub_u32 s37, s37, 1\n"
    "s_cbranch_scc1 .Lfd_D8\n"
    "s_sleep 1\n"
    ".Lfd_G8:\n"
    "global_load_dwordx4 v[32:35], v29, s[40:41] offset:0 sc1\n"
    "global_load_dwordx4 v[36:39], v29, s[40:41] offset:1024 sc1\n"
    "s_waitcnt vmcnt(0)\n"
    ".Lfd_C8:\n"
    "v_bitop3_b32 v33, v33, v35, s36 bitop3:0x7e\n"
    "v_bitop3_b32 v37, v37, v39, s36 bitop3:0x7e\n"
    "v_or_b32 v33, v33, v37\n"
    "v_cmpx_eq_u32 vcc, 0, v33\n"
    "s_andn2_b64 exec, s[42:43], exec\n"
    "s_mov_b64 s[42:43], exec\n"
    "s_cbranch_execnz .Lfd_R8\n"
    ".Lfd_D8:\n"
    "s_mov_b64 exec, -1\n"
    "s_mul_i32 s46, s28, 272\n"
    "s_mov_b32 s50, 0xffff0000\n"
    "v_lshlrev_b32 v40, 16, v32\n"
    "v_and_b32 v41, s50, v32\n"
    "v_lshlrev_b32 v42, 16, v34\n"
    "v_and_b32 v43, s50, v34\n"
    "v_lshlrev_b32 v44, 16, v36\n"
    "v_and_b32 v45, s50, v36\n"
    "v_lshlrev_b32 v46, 16, v38\n"
    "v_and_b32 v47, s50, v38\n"
    "v_pk_add_f32 v[40:41], v[40:41], v[44:45]\n"
    "v_pk_add_f32 v[42:43], v[42:43], v[46:47]\n"
    "v_lshrrev_b32 v36, 5, v1\n"
    "v_and_b32 v37, 31, v1\n"
    "v_permlane32_swap_b32 v40, v42\n"
    "v_permlane32_swap_b32 v41, v43\n"
    "v_lshl_add_u32 v36, v37, 1, v36\n"
    "v_pk_add_f32 v[40:41], v[40:41], v[42:43]\n"
    "v_lshl_add_u32 v36, v36, 2, s46\n"
    "v_cvt_pk_bf16_f32 v32, v40, v41\n"
    "ds_write_b32 v36, v32 offset:58368\n"
    "s_lshl_b32 s46, s35, 12\n"
    "s_add_u32 s46, s46, 266240\n"
    "v_bfe_u32 v79, v1, 1, 1\n"
    "v_lshlrev_b32 v79, 4, v79\n"
    "v_and_b32 v80, 1, v1\n"
    "v_lshl_add_u32 v79, v80, 3, v79\n"
    "v_bfe_u32 v80, v1, 2, 2\n"
    "v_lshl_add_u32 v79, v80, 1, v79\n"
    "v_lshl_add_u32 v79, v6, 6, v79\n"
    "v_add_u32 v79, 59840, v79\n"
    "s_mov_b32 s50, 0xffff0000\n"
    "s_add_u32 s92, s12, s46\n"
    "s_addc_u32 s93, s13, 0\n"
    "s_mul_i32 s46, s35, 12288\n"
    "s_add_u32 s46, s46, 528640\n"
    "s_add_u32 s94, s12, s46\n"
    "s_addc_u32 s95, s13, 0\n"
    "v_mul_u32_u24 v81, 384, v1\n"
    "v_and_b32 v82, 1, v6\n"
    "v_lshlrev_b32 v82, 3, v82\n"
    "v_add_u32 v82, 59696, v82\n"
    "v_lshlrev_b32 v83, 2, v1\n"
    "v_add_u32 v83, 59968, v83\n"
    "v_mov_b32 v84, 75328\n"
    "v_and_b32 v28, 16, v1\n"
    "s_cmp_eq_u32 s28, 1\n"
    "s_cbranch_scc1 .Lfd_S1\n"
    "s_cmp_eq_u32 s28, 2\n"
    "s_cbranch_scc1 .Lfd_S2\n"
    "s_cmp_eq_u32 s28, 0\n"
    "s_cbranch_scc0 .Lfd_NOXM\n"
    "v_mov_b64 v[56:57], 0\n"
    "v_mov_b64 v[58:59], 0\n"
    "v_mov_b64 v[60:61], 0\n"
    "v_mov_b64 v[62:63], 0\n"
    "v_mov_b64 v[64:65], 0\n"
    "v_mov_b64 v[66:67], 0\n"
    "v_mov_b64 v[68:69], 0\n"
    "v_mov_b64 v[70:71], 0\n"
    "s_mov_b32 exec_lo, 0xf000f\n"
    "s_mov_b32 exec_hi, 0xf000f\n"
    "v_mov_b64 v[56:57], v[8:9]\n"
    "v_mov_b64 v[58:59], v[10:11]\n"
    "v_mov_b64 v[60:61], v[12:13]\n"
    "v_mov_b64 v[62:63], v[14:15]\n"
    "v_mov_b64 v[64:65], v[16:17]\n"
    "v_mov_b64 v[66:67], v[18:19]\n"
    "v_mov_b64 v[68:69], v[20:21]\n"
    "v_mov_b64 v[70:71], v[22:23]\n"
    "s_mov_b64 exec, -1\n"
    "v_mov_b64 v[96:97], 0\n"
    "v_mov_b64 v[98:99], 0\n"
    "v_mov_b64 v[100:101], 0\n"
    "v_mov_b64 v[102:103], 0\n"
    "v_mov_b64 v[104:105], 0\n"
    "v_mov_b64 v[106:107], 0\n"
    "v_mov_b64 v[108:109], 0\n"
    "v_mov_b64 v[110:111], 0\n"
    "s_mov_b32 exec_lo, 0xf000f0\n"
    "s_mov_b32 exec_hi, 0xf000f0\n"
    "v_mov_b64 v[96:97], v[8:9]\n"
    "v_mov_b64 v[98:99], v[10:11]\n"
    "v_mov_b64 v[100:101], v[12:13]\n"
    "v_mov_b64 v[102:103], v[14:15]\n"
    "v_mov_b64 v[104:105], v[16:17]\n"
    "v_mov_b64 v[106:107], v[18:19]\n"
    "v_mov_b64 v[108:109], v[20:21]\n"
    "v_mov_b64 v[110:111], v[22:23]\n"
    "s_mov_b64 exec, -1\n"
    "v_mov_b64 v[112:113], 0\n"
    "v_mov_b64 v[114:115], 0\n"
    "v_mov_b64 v[116:117], 0\n"
    "v_mov_b64 v[118:119], 0\n"
    "v_mov_b64 v[120:121], 0\n"
    "v_mov_b64 v[122:123], 0\n"
    "v_mov_b64 v[124:125], 0\n"
    "v_mov_b64 v[126:127], 0\n"
    "s_mov_b32 exec_lo, 0xf000f00\n"
    "s_mov_b32 exec_hi, 0xf000f00\n"
    "v_mov_b64 v[112:113], v[8:9]\n"
    "v_mov_b64 v[114:115], v[10:11]\n"
    "v_mov_b64 v[116:117], v[12:13]\n"
    "v_mov_b64 v[118:119], v[14:15]\n"
    "v_mov_b64 v[120:121], v[16:17]\n"
    "v_mov_b64 v[122:123], v[18:19]\n"
    "v_mov_b64 v[124:125], v[20:21]\n"
    "v_mov_b64 v[126:127], v[22:23]\n"
    "s_mov_b64 exec, -1\n"
    "s_mov_b32 exec_lo, 0x0fff0fff\n"
    "s_mov_b32 exec_hi, 0x0fff0fff\n"
    "v_mov_b64 v[8:9], 0\n"
    "v_mov_b64 v[10:11], 0\n"
    "v_mov_b64 v[12:13], 0\n"
    "v_mov_b64 v[14:15], 0\n"
    "v_mov_b64 v[16:17], 0\n"
    "v_mov_b64 v[18:19], 0\n"
    "v_mov_b64 v[20:21], 0\n"
    "v_mov_b64 v[22:23], 0\n"
    "s_mov_b64 exec, -1\n"
    "v_mov_b32 v85, v3\n"
    "v_xor_b32 v86, 32, v3\n"
    "v_xor_b32 v87, 64, v3\n"
    "v_xor_b32 v88, 96, v3\n"
    "v_bfe_u32 v89, v1, 3, 1\n"
    "v_lshl_add_u32 v30, v89, 12, v30\n"
    "v_bfe_u32 v89, v1, 2, 1\n"
    "v_lshl_add_u32 v30, v89, 7, v30\n"
    ".Lfd_NOXM:\n"
    "s_cmp_lg_u32 s28, 3\n"
    "s_cbranch_scc1 .Lfd_NOGATE\n"
    "s_mov_b32 s46, 0xffff0000\n"
    "v_mov_b32 v32, 0\n"
    "v_mov_b32 v33, 0\n"
    "v_mov_b32 v34, 0x3f803f80\n"
    "v_lshlrev_b32 v36, 16, v72\n"
    "v_and_b32 v37, s46, v72\n"
    "v_lshlrev_b32 v38, 16, v64\n"
    "v_and_b32 v39, s46, v64\n"
    "v_lshlrev_b32 v40, 16, v68\n"
    "v_and_b32 v41, s46, v68\n"
    "v_pk_mul_f32 v[38:39], v[38:39], v[36:37]\n"
    "v_pk_mul_f32 v[40:41], v[40:41], v[36:37]\n"
    "v_cvt_pk_bf16_f32 v38, v38, v39\n"
    "v_cvt_pk_bf16_f32 v40, v40, v41\n"
    "v_dot2_f32_bf16 v32, v38, v34, v32\n"
    "v_dot2_f32_bf16 v33, v40, v34, v33\n"
    "v_lshlrev_b32 v36, 16, v73\n"
    "v_and_b32 v37, s46, v73\n"
    "v_lshlrev_b32 v38, 16, v65\n"
    "v_and_b32 v39, s46, v65\n"
    "v_lshlrev_b32 v40, 16, v69\n"
    "v_and_b32 v41, s46, v69\n"
    "v_pk_mul_f32 v[38:39], v[38:39], v[36:37]\n"
    "v_pk_mul_f32 v[40:41], v[40:41], v[36:37]\n"
    "v_cvt_pk_bf16_f32 v38, v38, v39\n"
    "v_cvt_pk_bf16_f32 v40, v40, v41\n"
    "v_dot2_f32_bf16 v32, v38, v34, v32\n"
    "v_dot2_f32_bf16 v33, v40, v34, v33\n"
    "v_lshlrev_b32 v36, 16, v74\n"
    "v_and_b32 v37, s46, v74\n"
    "v_lshlrev_b32 v38, 16, v66\n"
    "v_and_b32 v39, s46, v66\n"
    "v_lshlrev_b32 v40, 16, v70\n"
    "v_and_b32 v41, s46, v70\n"
    "v_pk_mul_f32 v[38:39], v[38:39], v[36:37]\n"
    "v_pk_mul_f32 v[40:41], v[40:41], v[36:37]\n"
    "v_cvt_pk_bf16_f32 v38, v38, v39\n"
    "v_cvt_pk_bf16_f32 v40, v40, v41\n"
    "v_dot2_f32_bf16 v32, v38, v34, v32\n"
    "v_dot2_f32_bf16 v33, v40, v34, v33\n"
    "v_lshlrev_b32 v36, 16, v75\n"
    "v_and_b32 v37, s46, v75\n"
    "v_lshlrev_b32 v38, 16, v67\n"
    "v_and_b32 v39, s46, v67\n"
    "v_lshlrev_b32 v40, 16, v71\n"
    "v_and_b32 v41, s46, v71\n"
    "v_pk_mul_f32 v[38:39], v[38:39], v[36:37]\n"
    "v_pk_mul_f32 v[40:41], v[40:41], v[36:37]\n"
    "v_cvt_pk_bf16_f32 v38, v38, v39\n"
    "v_cvt_pk_bf16_f32 v40, v40, v41\n"
    "v_dot2_f32_bf16 v32, v38, v34, v32\n"
    "v_dot2_f32_bf16 v33, v40, v34, v33\n"
    "s_nop 2\n"
    "v_add_f32_dpp v32, v32, v32 row_ror:8 row_mask:0xf bank_mask:0xf\n"
    "v_add_f32_dpp v33, v33, v33 row_ror:8 row_mask:0xf bank_mask:0xf\n"
    "s_nop 0\n"
    "v_add_f32_dpp v32, v32, v32 row_ror:4 row_mask:0xf bank_mask:0xf\n"
    "v_add_f32_dpp v33, v33, v33 row_ror:4 row_mask:0xf bank_mask:0xf\n"
    "s_nop 0\n"
    "v_add_f32_dpp v32, v32, v32 row_ror:2 row_mask:0xf bank_mask:0xf\n"
    "v_add_f32_dpp v33, v33, v33 row_ror:2 row_mask:0xf bank_mask:0xf\n"
    "s_nop 0\n"
    "v_add_f32_dpp v32, v32, v32 row_ror:1 row_mask:0xf bank_mask:0xf\n"
    "v_add_f32_dpp v33, v33, v33 row_ror:1 row_mask:0xf bank_mask:0xf\n"
    "s_nop 0\n"
    "s_nop 0\n"
    "v_permlane16_swap_b32 v32, v33\n"
    "v_add_f32 v32, v32, v33\n"
    "v_mov_b32 v33, v32\n"
    "s_nop 1\n"
    "v_permlane32_swap_b32 v32, v33\n"
    "v_add_f32 v32, v32, v33\n"
    "v_cvt_pk_bf16_f32 v32, v32, v32\n"
    "v_and_b32 v32, s46, v32\n"
    "v_mul_f32 v32, 0xbfb8aa3b, v32\n"
    "v_exp_f32 v32, v32\n"
    "s_nop 0\n"
    "v_add_f32 v32, 1.0, v32\n"
    "v_rcp_f32 v32, v32\n"
    "s_nop 0\n"
    "v_cvt_pk_bf16_f32 v32, v32, v32\n"
    "v_and_b32 v32, s46, v32\n"
    "v_sub_f32 v33, 1.0, v32\n"
    "v_cvt_pk_bf16_f32 v33, v33, v33\n"
    "v_and_b32 v33, s46, v33\n"
    "v_lshrrev_b32 v34, 1, v1\n"
    "v_add_u32 v34, 59696, v34\n"
    "s_mov_b64 exec, 0x10001\n"
    "ds_write_b64 v34, v[32:33]\n"
    "s_mov_b64 exec, -1\n"
    ".Lfd_NOGATE:\n"
    "v_lshlrev_b32 v54, 10, v6\n"
    "v_lshl_or_b32 v54, v7, 4, v54\n"
    "s_lshl_b32 s46, s48, 7\n"
    "v_xor_b32 v55, s46, v54\n"
    "s_lshl1_add_u32 s46, s35, s48\n"
    "s_lshl_b32 s46, s46, 15\n"
    "s_lshl_b32 s47, s57, 8\n"
    "s_add_u32 s46, s46, s47\n"
    "s_add_u32 s40, s20, s46\n"
    "s_addc_u32 s41, s21, 0\n"
    "s_cmp_lt_u32 s35, 56\n"
    "s_cselect_b32 s46, s35, 0\n"
    "s_lshl1_add_u32 s46, s46, s48\n"
    "s_lshl_b32 s46, s46, 14\n"
    "s_add_u32 s46, s46, s47\n"
    "s_add_u32 s44, s22, s46\n"
    "s_addc_u32 s45, s23, 0\n"
    "s_lshl_b32 s46, s48, 3\n"
    "v_lshl_add_u32 v77, v1, 2, s46\n"
    "s_add_u32 s53, s29, 1\n"
    "s_and_b32 s53, s53, 3\n"
    "s_lshr_b32 s46, s29, 2\n"
    "s_lshl1_add_u32 s53, s53, s46\n"
    "s_lshr_b32 s47, s30, 1\n"
    "s_lshl3_add_u32 s53, s53, s47\n"
    "s_lshl_b32 s46, s53, 13\n"
    "s_add_u32 s46, s46, 1318912\n"
    "s_and_b32 s89, s30, 1\n"
    "s_lshl_b32 s47, s89, 6\n"
    "s_add_u32 s46, s46, s47\n"
    "s_add_u32 s60, s12, s46\n"
    "s_addc_u32 s61, s13, 0\n"
    "s_and_b32 s46, s53, 1\n"
    "s_lshl_b32 s46, s46, 10\n"
    "s_lshr_b32 s47, s53, 1\n"
    "s_lshl_b32 s47, s47, 5\n"
    "s_add_u32 s46, s46, s47\n"
    "s_lshl_b32 s47, s89, 4\n"
    "s_add_u32 s46, s46, s47\n"
    "s_lshl2_add_u32 s53, s53, s89\n"
    "v_lshlrev_b32 v76, 2, v7\n"
    "v_lshl_add_u32 v76, v6, 1, v76\n"
    "v_lshrrev_b32 v90, 2, v1\n"
    "v_and_b32 v91, 3, v1\n"
    "v_lshlrev_b32 v91, 4, v91\n"
    "v_lshl_or_b32 v90, v90, 7, v91\n"
    "s_add_u32 s62, s24, s46\n"
    "s_addc_u32 s63, s25, 0\n"
    "s_lshl_b32 s46, s48, 13\n"
    "s_add_u32 m0, s46, 32768\n"
    "s_lshl_b32 s46, s48, 12\n"
    "s_mov_b32 s37, 2097152\n"
    "s_branch .Lfd_G9\n"
    ".Lfd_R9:\n"
    "s_sub_u32 s37, s37, 1\n"
    "s_cbranch_scc1 .Lfd_D9\n"
    "s_sleep 2\n"
    ".Lfd_G9:\n"
    "ds_read_b128 v[32:35], v28 offset:59600\n"
    "s_waitcnt lgkmcnt(0)\n"
    "v_max3_u32 v32, v32, v33, v34\n"
    "v_max_u32 v32, v32, v35\n"
    "v_cmp_eq_u32 vcc, s49, v32\n"
    "s_cbranch_vccnz .Lfd_R9\n"
    ".Lfd_D9:\n"
    "s_cmp_eq_u32 s28, 0\n"
    "s_cbranch_scc0 .Lfd_S3\n"
    "s_cmp_lt_u32 s54, 28\n"
    "s_cbranch_scc0 .Lfd_END\n"
    "s_mov_b32 m0, 16384\n"
    "s_nop 0\n"
    "global_load_lds_dwordx4 v52, s[90:91] nt\n"
    "s_add_u32 m0, m0, 0x400\n"
    "s_add_u32 s90, s90, 28672\n"
    "s_addc_u32 s91, s91, 0\n"
    "global_load_lds_dwordx4 v53, s[90:91] nt\n"
    "s_add_u32 m0, m0, 0x400\n"
    "s_add_u32 s90, s90, 28672\n"
    "s_addc_u32 s91, s91, 0\n"
    "global_load_lds_dwordx4 v52, s[90:91] nt\n"
    "s_add_u32 m0, m0, 0x400\n"
    "s_add_u32 s90, s90, 28672\n"
    "s_addc_u32 s91, s91, 0\n"
    "global_load_lds_dwordx4 v53, s[90:91] nt\n"
    "s_add_u32 m0, m0, 0x400\n"
    "s_add_u32 s90, s90, 28672\n"
    "s_addc_u32 s91, s91, 0\n"
    "global_load_lds_dwordx4 v52, s[90:91] nt\n"
    "s_add_u32 m0, m0, 0x400\n"
    "s_add_u32 s90, s90, 28672\n"
    "s_addc_u32 s91, s91, 0\n"
    "global_load_lds_dwordx4 v53, s[90:91] nt\n"
    "s_add_u32 m0, m0, 0x400\n"
    "s_add_u32 s90, s90, 28672\n"
    "s_addc_u32 s91, s91, 0\n"
    "global_load_lds_dwordx4 v52, s[90:91] nt\n"
    "s_add_u32 m0, m0, 0x400\n"
    "s_add_u32 s90, s90, 28672\n"
    "s_addc_u32 s91, s91, 0\n"
    "global_load_lds_dwordx4 v53, s[90:91] nt\n"
    "s_add_u32 m0, m0, 0x400\n"
    "s_add_u32 s90, s90, 28672\n"
    "s_addc_u32 s91, s91, 0\n"
    "global_load_lds_dwordx4 v52, s[90:91] nt\n"
    "s_add_u32 m0, m0, 0x400\n"
    "s_add_u32 s90, s90, 28672\n"
    "s_addc_u32 s91, s91, 0\n"
    "global_load_lds_dwordx4 v53, s[90:91] nt\n"
    "s_add_u32 m0, m0, 0x400\n"
    "s_add_u32 s90, s90, 28672\n"
    "s_addc_u32 s91, s91, 0\n"
    "global_load_lds_dwordx4 v52, s[90:91] nt\n"
    "s_add_u32 m0, m0, 0x400\n"
    "s_add_u32 s90, s90, 28672\n"
    "s_addc_u32 s91, s91, 0\n"
    "global_load_lds_dwordx4 v53, s[90:91] nt\n"
    "s_add_u32 m0, m0, 0x400\n"
    "s_add_u32 s90, s90, 28672\n"
    "s_addc_u32 s91, s91, 0\n"
    "global_load_lds_dwordx4 v52, s[90:91] nt\n"
    "s_add_u32 m0, m0, 0x400\n"
    "s_add_u32 s90, s90, 28672\n"
    "s_addc_u32 s91, s91, 0\n"
    "global_load_lds_dwordx4 v53, s[90:91] nt\n"
    "s_add_u32 m0, m0, 0x400\n"
    "s_add_u32 s90, s90, 28672\n"
    "s_addc_u32 s91, s91, 0\n"
    "global_load_lds_dwordx4 v52, s[90:91] nt\n"
    "s_add_u32 m0, m0, 0x400\n"
    "s_add_u32 s90, s90, 28672\n"
    "s_addc_u32 s91, s91, 0\n"
    "global_load_lds_dwordx4 v53, s[90:91] nt\n"
    "s_waitcnt vmcnt(12)\n"
    "ds_read_b64_tr_b16 v[32:33], v85 offset:16384\n"
    "ds_read_b64_tr_b16 v[34:35], v85 offset:16896\n"
    "ds_read_b64_tr_b16 v[36:37], v86 offset:16384\n"
    "ds_read_b64_tr_b16 v[38:39], v86 offset:16896\n"
    "ds_read_b64_tr_b16 v[40:41], v87 offset:16384\n"
    "ds_read_b64_tr_b16 v[42:43], v87 offset:16896\n"
    "ds_read_b64_tr_b16 v[44:45], v88 offset:16384\n"
    "ds_read_b64_tr_b16 v[46:47], v88 offset:16896\n"
    "s_waitcnt lgkmcnt(6)\n"
    "v_mfma_f32_16x16x32_bf16 v[48:51], v[32:35], v[56:59], 0\n"
    "s_waitcnt lgkmcnt(4)\n"
    "v_mfma_f32_16x16x32_bf16 v[48:51], v[36:39], v[96:99], v[48:51]\n"
    "s_waitcnt lgkmcnt(2)\n"
    "v_mfma_f32_16x16x32_bf16 v[48:51], v[40:43], v[112:115], v[48:51]\n"
    "s_waitcnt lgkmcnt(0)\n"
    "v_mfma_f32_16x16x32_bf16 v[48:51], v[44:47], v[8:11], v[48:51]\n"
    "s_waitcnt vmcnt(8)\n"
    "ds_read_b64_tr_b16 v[32:33], v85 offset:20480\n"
    "ds_read_b64_tr_b16 v[34:35], v85 offset:20992\n"
    "ds_read_b64_tr_b16 v[36:37], v86 offset:20480\n"
    "ds_read_b64_tr_b16 v[38:39], v86 offset:20992\n"
    "ds_read_b64_tr_b16 v[40:41], v87 offset:20480\n"
    "ds_read_b64_tr_b16 v[42:43], v87 offset:20992\n"
    "ds_read_b64_tr_b16 v[44:45], v88 offset:20480\n"
    "ds_read_b64_tr_b16 v[46:47], v88 offset:20992\n"
    "s_waitcnt lgkmcnt(6)\n"
    "v_mfma_f32_16x16x32_bf16 v[48:51], v[32:35], v[60:63], v[48:51]\n"
    "s_waitcnt lgkmcnt(4)\n"
    "v_mfma_f32_16x16x32_bf16 v[48:51], v[36:39], v[100:103], v[48:51]\n"
    "s_waitcnt lgkmcnt(2)\n"
    "v_mfma_f32_16x16x32_bf16 v[48:51], v[40:43], v[116:119], v[48:51]\n"
    "s_waitcnt lgkmcnt(0)\n"
    "v_mfma_f32_16x16x32_bf16 v[48:51], v[44:47], v[12:15], v[48:51]\n"
    "s_waitcnt vmcnt(4)\n"
    "ds_read_b64_tr_b16 v[32:33], v85 offset:24576\n"
    "ds_read_b64_tr_b16 v[34:35], v85 offset:25088\n"
    "ds_read_b64_tr_b16 v[36:37], v86 offset:24576\n"
    "ds_read_b64_tr_b16 v[38:39], v86 offset:25088\n"
    "ds_read_b64_tr_b16 v[40:41], v87 offset:24576\n"
    "ds_read_b64_tr_b16 v[42:43], v87 offset:25088\n"
    "ds_read_b64_tr_b16 v[44:45], v88 offset:24576\n"
    "ds_read_b64_tr_b16 v[46:47], v88 offset:25088\n"
    "s_waitcnt lgkmcnt(6)\n"
    "v_mfma_f32_16x16x32_bf16 v[48:51], v[32:35], v[64:67], v[48:51]\n"
    "s_waitcnt lgkmcnt(4)\n"
    "v_mfma_f32_16x16x32_bf16 v[48:51], v[36:39], v[104:107], v[48:51]\n"
    "s_waitcnt lgkmcnt(2)\n"
    "v_mfma_f32_16x16x32_bf16 v[48:51], v[40:43], v[120:123], v[48:51]\n"
    "s_waitcnt lgkmcnt(0)\n"
    "v_mfma_f32_16x16x32_bf16 v[48:51], v[44:47], v[16:19], v[48:51]\n"
    "s_waitcnt vmcnt(0)\n"
    "ds_read_b64_tr_b16 v[32:33], v85 offset:28672\n"
    "ds_read_b64_tr_b16 v[34:35], v85 offset:29184\n"
    "ds_read_b64_tr_b16 v[36:37], v86 offset:28672\n"
    "ds_read_b64_tr_b16 v[38:39], v86 offset:29184\n"
    "ds_read_b64_tr_b16 v[40:41], v87 offset:28672\n"
    "ds_read_b64_tr_b16 v[42:43], v87 offset:29184\n"
    "ds_read_b64_tr_b16 v[44:45], v88 offset:28672\n"
    "ds_read_b64_tr_b16 v[46:47], v88 offset:29184\n"
    "s_waitcnt lgkmcnt(6)\n"
    "v_mfma_f32_16x16x32_bf16 v[48:51], v[32:35], v[68:71], v[48:51]\n"
    "s_waitcnt lgkmcnt(4)\n"
    "v_mfma_f32_16x16x32_bf16 v[48:51], v[36:39], v[108:111], v[48:51]\n"
    "s_waitcnt lgkmcnt(2)\n"
    "v_mfma_f32_16x16x32_bf16 v[48:51], v[40:43], v[124:127], v[48:51]\n"
    "s_waitcnt lgkmcnt(0)\n"
    "v_mfma_f32_16x16x32_bf16 v[48:51], v[44:47], v[20:23], v[48:51]\n"
    "s_nop 7\n"
    "v_cndmask_b32 v24, v48, v50, s[58:59]\n"
    "v_cndmask_b32 v26, v49, v51, s[58:59]\n"
    "global_store_dwordx4 v30, v[24:27], s[38:39] sc1\n"
    "s_endpgm\n"
    ".Lfd_S3:\n"
    "s_mov_b32 m0, 32768\n"
    "v_lshlrev_b32 v77, 2, v1\n"
    "v_xor_b32 v55, 128, v55\n"
    "s_sub_u32 s40, s40, 32768\n"
    "s_subb_u32 s41, s41, 0\n"
    "s_sub_u32 s44, s44, 16384\n"
    "s_subb_u32 s45, s45, 0\n"
    "s_sleep 24\n"
    "global_load_lds_dwordx4 v54, s[40:41] nt\n"
    "s_add_u32 m0, m0, 0x400\n"
    "v_xor_b32 v54, 4128, v54\n"
    "global_load_lds_dwordx4 v54, s[40:41] nt\n"
    "s_add_u32 m0, m0, 0x400\n"
    "v_xor_b32 v54, 12384, v54\n"
    "global_load_lds_dwordx4 v54, s[40:41] nt\n"
    "s_add_u32 m0, m0, 0x400\n"
    "v_xor_b32 v54, 4128, v54\n"
    "global_load_lds_dwordx4 v54, s[40:41] nt\n"
    "s_add_u32 m0, m0, 0x400\n"
    "v_xor_b32 v54, 28896, v54\n"
    "global_load_lds_dwordx4 v54, s[40:41] nt\n"
    "s_add_u32 m0, m0, 0x400\n"
    "v_xor_b32 v54, 4128, v54\n"
    "global_load_lds_dwordx4 v54, s[40:41] nt\n"
    "s_add_u32 m0, m0, 0x400\n"
    "v_xor_b32 v54, 12384, v54\n"
    "global_load_lds_dwordx4 v54, s[40:41] nt\n"
    "s_add_u32 m0, m0, 0x400\n"
    "v_xor_b32 v54, 4128, v54\n"
    "global_load_lds_dwordx4 v54, s[40:41] nt\n"
    "s_add_u32 m0, m0, 0x400\n"
    "v_xor_b32 v54, 61664, v54\n"
    "global_load_lds_dwordx4 v54, s[40:41] nt\n"
    "s_add_u32 m0, m0, 0x400\n"
    "v_xor_b32 v54, 4128, v54\n"
    "global_load_lds_dwordx4 v54, s[40:41] nt\n"
    "s_add_u32 m0, m0, 0x400\n"
    "v_xor_b32 v54, 12384, v54\n"
    "global_load_lds_dwordx4 v54, s[40:41] nt\n"
    "s_add_u32 m0, m0, 0x400\n"
    "v_xor_b32 v54, 4128, v54\n"
    "global_load_lds_dwordx4 v54, s[40:41] nt\n"
    "s_add_u32 m0, m0, 0x400\n"
    "v_xor_b32 v54, 28896, v54\n"
    "global_load_lds_dwordx4 v54, s[40:41] nt\n"
    "s_add_u32 m0, m0, 0x400\n"
    "v_xor_b32 v54, 4128, v54\n"
    "global_load_lds_dwordx4 v54, s[40:41] nt\n"
    "s_add_u32 m0, m0, 0x400\n"
    "v_xor_b32 v54, 12384, v54\n"
    "global_load_lds_dwordx4 v54, s[40:41] nt\n"
    "s_add_u32 m0, m0, 0x400\n"
    "v_xor_b32 v54, 4128, v54\n"
    "global_load_lds_dwordx4 v54, s[40:41] nt\n"
    "s_add_u32 m0, m0, 0x400\n"
    "v_xor_b32 v54, 127200, v54\n"
    "global_load_lds_dwordx4 v55, s[44:45] nt\n"
    "s_add_u32 m0, m0, 0x400\n"
    "v_xor_b32 v55, 4128, v55\n"
    "global_load_lds_dwordx4 v55, s[44:45] nt\n"
    "s_add_u32 m0, m0, 0x400\n"
    "v_xor_b32 v55, 12384, v55\n"
    "global_load_lds_dwordx4 v55, s[44:45] nt\n"
    "s_add_u32 m0, m0, 0x400\n"
    "v_xor_b32 v55, 4128, v55\n"
    "global_load_lds_dwordx4 v55, s[44:45] nt\n"
    "s_add_u32 m0, m0, 0x400\n"
    "v_xor_b32 v55, 28896, v55\n"
    "global_load_lds_dwordx4 v55, s[44:45] nt\n"
    "s_add_u32 m0, m0, 0x400\n"
    "v_xor_b32 v55, 4128, v55\n"
    "global_load_lds_dwordx4 v55, s[44:45] nt\n"
    "s_add_u32 m0, m0, 0x400\n"
    "v_xor_b32 v55, 12384, v55\n"
    "global_load_lds_dwordx4 v55, s[44:45] nt\n"
    "s_add_u32 m0, m0, 0x400\n"
    "v_xor_b32 v55, 4128, v55\n"
    "global_load_lds_dwordx4 v55, s[44:45] nt\n"
    "s_waitcnt vmcnt(16)\n"
    "s_mov_b64 exec, 15\n"
    "ds_write_b32 v77, v31 offset:59680\n"
    "s_waitcnt vmcnt(0)\n"
    "ds_write_b32 v77, v31 offset:59632\n"
    "s_mov_b64 exec, -1\n"
    "s_cmp_eq_u32 s28, 3\n"
    "s_cbranch_scc0 .Lfd_END\n"
    "s_cmp_lt_u32 s30, 16\n"
    "s_cbranch_scc0 .Lfd_END\n"
    "s_branch .Lfd_LAST\n"
    ".Lfd_S1:\n"
    "v_mov_b32 v32, 0\n"
    "s_cmp_lt_u32 s35, 56\n"
    "s_cbranch_scc0 .Lfd_HPUT\n"
    "s_sleep 4\n"
    "s_mov_b32 s37, 2097152\n"
    "s_branch .Lfd_G10\n"
    ".Lfd_R10:\n"
    "s_sub_u32 s37, s37, 1\n"
    "s_cbranch_scc1 .Lfd_D10\n"
    "s_sleep 1\n"
    ".Lfd_G10:\n"
    "ds_read_b128 v[32:35], v31 offset:59680\n"
    "s_waitcnt lgkmcnt(0)\n"
    "v_max3_u32 v32, v32, v33, v34\n"
    "v_max_u32 v32, v32, v35\n"
    "v_cmp_eq_u32 vcc, s49, v32\n"
    "s_cbranch_vccnz .Lfd_R10\n"
    ".Lfd_D10:\n"
    "s_mov_b64 s[42:43], exec\n"
    "s_mov_b32 s37, 131072\n"
    "global_load_dwordx4 v[40:43], v29, s[92:93] offset:0 sc1\n"
    "global_load_dwordx4 v[44:47], v29, s[92:93] offset:1024 sc1\n"
    "global_load_dwordx4 v[48:51], v29, s[92:93] offset:2048 sc1\n"
    "global_load_dwordx4 v[52:55], v29, s[92:93] offset:3072 sc1\n"
    "s_sleep 7\n"
    ".Lfd_G11:\n"
    "global_load_dwordx4 v[56:59], v29, s[92:93] offset:0 sc1\n"
    "global_load_dwordx4 v[60:63], v29, s[92:93] offset:1024 sc1\n"
    "global_load_dwordx4 v[64:67], v29, s[92:93] offset:2048 sc1\n"
    "global_load_dwordx4 v[68:71], v29, s[92:93] offset:3072 sc1\n"
    "s_waitcnt vmcnt(4)\n"
    "v_bitop3_b32 v41, v41, v43, s36 bitop3:0x7e\n"
    "v_bitop3_b32 v45, v45, v47, s36 bitop3:0x7e\n"
    "v_bitop3_b32 v49, v49, v51, s36 bitop3:0x7e\n"
    "v_bitop3_b32 v53, v53, v55, s36 bitop3:0x7e\n"
    "v_or3_b32 v41, v41, v45, v49\n"
    "v_or_b32 v53, v53, v41\n"
    "v_cmpx_eq_u32 vcc, 0, v53\n"
    "v_add_f32 v40, v40, v44\n"
    "v_add_f32 v48, v48, v52\n"
    "v_add_f32 v42, v42, v46\n"
    "v_add_f32 v50, v50, v54\n"
    "v_add_f32 v40, v40, v48\n"
    "v_add_f32 v42, v42, v50\n"
    "v_cvt_pk_bf16_f32 v32, v40, v42\n"
    "s_andn2_b64 exec, s[42:43], exec\n"
    "s_mov_b64 s[42:43], exec\n"
    "s_cbranch_execz .Lfd_D11\n"
    "global_load_dwordx4 v[40:43], v29, s[92:93] offset:0 sc1\n"
    "global_load_dwordx4 v[44:47], v29, s[92:93] offset:1024 sc1\n"
    "global_load_dwordx4 v[48:51], v29, s[92:93] offset:2048 sc1\n"
    "global_load_dwordx4 v[52:55], v29, s[92:93] offset:3072 sc1\n"
    "s_waitcnt vmcnt(4)\n"
    "v_bitop3_b32 v57, v57, v59, s36 bitop3:0x7e\n"
    "v_bitop3_b32 v61, v61, v63, s36 bitop3:0x7e\n"
    "v_bitop3_b32 v65, v65, v67, s36 bitop3:0x7e\n"
    "v_bitop3_b32 v69, v69, v71, s36 bitop3:0x7e\n"
    "v_or3_b32 v57, v57, v61, v65\n"
    "v_or_b32 v69, v69, v57\n"
    "v_cmpx_eq_u32 vcc, 0, v69\n"
    "v_add_f32 v56, v56, v60\n"
    "v_add_f32 v64, v64, v68\n"
    "v_add_f32 v58, v58, v62\n"
    "v_add_f32 v66, v66, v70\n"
    "v_add_f32 v56, v56, v64\n"
    "v_add_f32 v58, v58, v66\n"
    "v_cvt_pk_bf16_f32 v32, v56, v58\n"
    "s_andn2_b64 exec, s[42:43], exec\n"
    "s_mov_b64 s[42:43], exec\n"
    "s_cbranch_execz .Lfd_D11\n"
    "s_sub_u32 s37, s37, 1\n"
    "s_cbranch_scc0 .Lfd_G11\n"
    ".Lfd_D11:\n"
    "s_mov_b64 exec, -1\n"
    "v_mov_b32 v33, v32\n"
    "s_nop 1\n"
    "v_permlane32_swap_b32 v32, v33\n"
    "v_lshlrev_b32 v34, 16, v32\n"
    "v_and_b32 v35, s50, v32\n"
    "v_mul_f32 v36, 0xbfb8aa3b, v34\n"
    "v_mul_f32 v37, 0xbfb8aa3b, v35\n"
    "v_exp_f32 v36, v36\n"
    "v_exp_f32 v37, v37\n"
    "v_lshlrev_b32 v38, 16, v33\n"
    "v_add_f32 v36, 1.0, v36\n"
    "v_add_f32 v37, 1.0, v37\n"
    "v_rcp_f32 v36, v36\n"
    "v_rcp_f32 v37, v37\n"
    "v_and_b32 v39, s50, v33\n"
    "v_pk_mul_f32 v[34:35], v[34:35], v[36:37]\n"
    "v_cvt_pk_bf16_f32 v32, v34, v35\n"
    "v_lshlrev_b32 v34, 16, v32\n"
    "v_and_b32 v35, s50, v32\n"
    "v_pk_mul_f32 v[34:35], v[34:35], v[38:39]\n"
    "v_cvt_pk_bf16_f32 v32, v34, v35\n"
    ".Lfd_HPUT:\n"
    "s_mov_b32 exec_hi, 0\n"
    "ds_write_b16 v79, v32\n"
    "ds_write_b16_d16_hi v79, v32 offset:32\n"
    "s_mov_b64 exec, -1\n"
    "s_branch .Lfd_SJOIN\n"
    ".Lfd_S2:\n"
    "s_mov_b32 s37, 2097152\n"
    "s_branch .Lfd_G12\n"
    ".Lfd_R12:\n"
    "s_sub_u32 s37, s37, 1\n"
    "s_cbranch_scc1 .Lfd_D12\n"
    "s_sleep 2\n"
    ".Lfd_G12:\n"
    "ds_read_b32 v40, v82\n"
    "ds_read_b32 v42, v84\n"
    "s_waitcnt lgkmcnt(0)\n"
    "v_cmp_eq_u32 vcc, s49, v42\n"
    "v_cmp_eq_u32 s[42:43], s49, v40\n"
    "s_or_b64 vcc, vcc, s[42:43]\n"
    "s_cbranch_vccnz .Lfd_R12\n"
    ".Lfd_D12:\n"
    "s_mov_b32 s37, 2097152\n"
    "s_branch .Lfd_G13\n"
    ".Lfd_R13:\n"
    "s_sub_u32 s37, s37, 1\n"
    "s_cbranch_scc1 .Lfd_D13\n"
    "s_sleep 1\n"
    ".Lfd_G13:\n"
    "ds_read_b128 v[32:35], v31 offset:59680\n"
    "s_waitcnt lgkmcnt(0)\n"
    "v_max3_u32 v32, v32, v33, v34\n"
    "v_max_u32 v32, v32, v35\n"
    "v_cmp_eq_u32 vcc, s49, v32\n"
    "s_cbranch_vccnz .Lfd_R13\n"
    ".Lfd_D13:\n"
    "s_mov_b32 exec_lo, -1\n"
    "s_mov_b32 exec_hi, 0\n"
    "s_mov_b64 s[42:43], exec\n"
    "s_mov_b32 s37, 131072\n"
    "global_load_dwordx4 v[60:63], v81, s[94:95] sc1\n"
    "s_sleep 7\n"
    ".Lfd_G14:\n"
    "global_load_dwordx4 v[64:67], v81, s[94:95] sc1\n"
    "s_waitcnt vmcnt(1)\n"
    "v_bitop3_b32 v61, v61, v63, s36 bitop3:0x7e\n"
    "v_cmpx_eq_u32 vcc, 0, v61\n"
    "v_mov_b32 v56, v60\n"
    "v_mov_b32 v58, v62\n"
    "s_andn2_b64 exec, s[42:43], exec\n"
    "s_mov_b64 s[42:43], exec\n"
    "s_cbranch_execz .Lfd_D14\n"
    "global_load_dwordx4 v[60:63], v81, s[94:95] sc1\n"
    "s_waitcnt vmcnt(1)\n"
    "v_bitop3_b32 v65, v65, v67, s36 bitop3:0x7e\n"
    "v_cmpx_eq_u32 vcc, 0, v65\n"
    "v_mov_b32 v56, v64\n"
    "v_mov_b32 v58, v66\n"
    "s_andn2_b64 exec, s[42:43], exec\n"
    "s_mov_b64 s[42:43], exec\n"
    "s_cbranch_execz .Lfd_D14\n"
    "s_sub_u32 s37, s37, 1\n"
    "s_cbranch_scc0 .Lfd_G14\n"
    ".Lfd_D14:\n"
    "s_mov_b32 exec_lo, -1\n"
    "s_mov_b32 exec_hi, 0\n"
    "v_max_f32_dpp v36, v56, v56 row_ror:8 row_mask:0xf bank_mask:0xf\n"
    "s_nop 1\n"
    "v_max_f32_dpp v36, v36, v36 row_ror:4 row_mask:0xf bank_mask:0xf\n"
    "s_nop 1\n"
    "v_max_f32_dpp v36, v36, v36 row_ror:2 row_mask:0xf bank_mask:0xf\n"
    "s_nop 1\n"
    "v_max_f32_dpp v36, v36, v36 row_ror:1 row_mask:0xf bank_mask:0xf\n"
    "v_sub_f32 v36, v56, v36\n"
    "v_exp_f32 v36, v36\n"
    "s_waitcnt lgkmcnt(0)\n"
    "v_mul_f32 v37, v36, v58\n"
    "v_mul_f32 v36, v36, v40\n"
    "s_nop 0\n"
    "v_add_f32_dpp v37, v37, v37 row_ror:8 row_mask:0xf bank_mask:0xf\n"
    "s_nop 1\n"
    "v_add_f32_dpp v37, v37, v37 row_ror:4 row_mask:0xf bank_mask:0xf\n"
    "s_nop 1\n"
    "v_add_f32_dpp v37, v37, v37 row_ror:2 row_mask:0xf bank_mask:0xf\n"
    "s_nop 1\n"
    "v_add_f32_dpp v37, v37, v37 row_ror:1 row_mask:0xf bank_mask:0xf\n"
    "v_rcp_f32 v37, v37\n"
    "s_nop 0\n"
    "v_mul_f32 v36, v36, v37\n"
    "ds_write_b32 v83, v36\n"
    "s_mov_b64 exec, -1\n"
    ".Lfd_SJOIN:\n"
    "s_waitcnt lgkmcnt(0)\n"
    "s_endpgm\n"
    ".Lfd_LAST:\n"
    "s_mov_b32 s37, 2097152\n"
    "s_branch .Lfd_G15\n"
    ".Lfd_R15:\n"
    "s_sub_u32 s37, s37, 1\n"
    "s_cbranch_scc1 .Lfd_D15\n"
    "s_sleep 1\n"
    ".Lfd_G15:\n"
    "ds_read_b128 v[32:35], v31 offset:59664\n"
    "s_waitcnt lgkmcnt(0)\n"
    "v_max3_u32 v32, v32, v33, v34\n"
    "v_max_u32 v32, v32, v35\n"
    "v_cmp_eq_u32 vcc, s49, v32\n"
    "s_cbranch_vccnz .Lfd_R15\n"
    ".Lfd_D15:\n"
    "s_sleep 6\n"
    "s_mov_b64 s[42:43], exec\n"
    "s_mov_b32 s37, 131072\n"
    "global_load_dwordx4 v[36:39], v90, s[60:61] offset:-4096 sc1\n"
    "global_load_dwordx4 v[40:43], v90, s[60:61] offset:-2048 sc1\n"
    "global_load_dwordx4 v[44:47], v90, s[60:61] offset:0 sc1\n"
    "global_load_dwordx4 v[48:51], v90, s[60:61] offset:2048 sc1\n"
    "s_sleep 7\n"
    ".Lfd_G16:\n"
    "global_load_dwordx4 v[96:99], v90, s[60:61] offset:-4096 sc1\n"
    "global_load_dwordx4 v[100:103], v90, s[60:61] offset:-2048 sc1\n"
    "global_load_dwordx4 v[104:107], v90, s[60:61] offset:0 sc1\n"
    "global_load_dwordx4 v[108:111], v90, s[60:61] offset:2048 sc1\n"
    "s_waitcnt vmcnt(4)\n"
    "v_bitop3_b32 v37, v37, v39, s36 bitop3:0x7e\n"
    "v_bitop3_b32 v41, v41, v43, s36 bitop3:0x7e\n"
    "v_bitop3_b32 v45, v45, v47, s36 bitop3:0x7e\n"
    "v_bitop3_b32 v49, v49, v51, s36 bitop3:0x7e\n"
    "v_or3_b32 v37, v37, v41, v45\n"
    "v_or_b32 v49, v49, v37\n"
    "v_cmpx_eq_u32 vcc, 0, v49\n"
    "v_add_f32 v36, v36, v40\n"
    "v_add_f32 v38, v38, v42\n"
    "v_add_f32 v44, v44, v48\n"
    "v_add_f32 v46, v46, v50\n"
    "v_add_f32 v32, v36, v44\n"
    "v_add_f32 v34, v38, v46\n"
    "s_andn2_b64 exec, s[42:43], exec\n"
    "s_mov_b64 s[42:43], exec\n"
    "s_cbranch_execz .Lfd_D16\n"
    "global_load_dwordx4 v[36:39], v90, s[60:61] offset:-4096 sc1\n"
    "global_load_dwordx4 v[40:43], v90, s[60:61] offset:-2048 sc1\n"
    "global_load_dwordx4 v[44:47], v90, s[60:61] offset:0 sc1\n"
    "global_load_dwordx4 v[48:51], v90, s[60:61] offset:2048 sc1\n"
    "s_waitcnt vmcnt(4)\n"
    "v_bitop3_b32 v97, v97, v99, s36 bitop3:0x7e\n"
    "v_bitop3_b32 v101, v101, v103, s36 bitop3:0x7e\n"
    "v_bitop3_b32 v105, v105, v107, s36 bitop3:0x7e\n"
    "v_bitop3_b32 v109, v109, v111, s36 bitop3:0x7e\n"
    "v_or3_b32 v97, v97, v101, v105\n"
    "v_or_b32 v109, v109, v97\n"
    "v_cmpx_eq_u32 vcc, 0, v109\n"
    "v_add_f32 v96, v96, v100\n"
    "v_add_f32 v98, v98, v102\n"
    "v_add_f32 v104, v104, v108\n"
    "v_add_f32 v106, v106, v110\n"
    "v_add_f32 v32, v96, v104\n"
    "v_add_f32 v34, v98, v106\n"
    "s_andn2_b64 exec, s[42:43], exec\n"
    "s_mov_b64 s[42:43], exec\n"
    "s_cbranch_execz .Lfd_D16\n"
    "s_sub_u32 s37, s37, 1\n"
    "s_cbranch_scc0 .Lfd_G16\n"
    ".Lfd_D16:\n"
    "s_mov_b64 exec, -1\n"
    "s_nop 1\n"
    "v_permlane16_swap_b32 v32, v34\n"
    "v_add_f32 v32, v32, v34\n"
    "v_mov_b32 v33, v32\n"
    "s_nop 1\n"
    "v_permlane32_swap_b32 v32, v33\n"
    "v_add_f32 v32, v32, v33\n"
    "s_nop 1\n"
    "v_add_f32_dpp v32, v32, v32 row_ror:8 row_mask:0xf bank_mask:0xf\n"
    "s_nop 1\n"
    "v_add_f32_dpp v32, v32, v32 row_ror:4 row_mask:0xf bank_mask:0xf\n"
    "v_cvt_pk_bf16_f32 v32, v32, v32\n"
    "s_mov_b64 exec, 0x000f000f\n"
    "global_store_short v76, v32, s[62:63]\n"
    "s_cmp_lg_u32 s53, 0\n"
    "s_cbranch_scc1 .Lfd_FIN\n"
    "s_mov_b64 exec, 1\n"
    "global_store_dword v31, v25, s[12:13] offset:0 sc1\n"
    ".Lfd_FIN:\n"
    ".Lfd_END:\n"
    "s_endpgm\n"
    ".Lfd_CODE_END:\n"
    :: "{s[0:1]}"(__builtin_amdgcn_kernarg_segment_ptr()), "{s2}"(blockIdx.x), "{v0}"(threadIdx.x),
       "s"((unsigned)(size_t)(__attribute__((address_space(3))) char*)smem)
    : "memory", "vcc", "scc", "v1", "v2", "v3", "v4", "v5", "v6", "v7", "v8", "v9", "v10", "v11", "v12", "v13", "v14", "v15", "v16", "v17", "v18", "v19", "v20", "v21", "v22", "v23", "v24", "v25", "v26", "v27", "v28", "v29", "v30", "v31", "v32", "v33", "v34", "v35", "v36", "v37", "v38", "v39", "v40", "v41", "v42", "v43", "v44", "v45", "v46", "v47", "v48", "v49", "v50", "v51", "v52", "v53", "v54", "v55", "v56", "v57", "v58", "v59", "v60", "v61", "v62", "v63", "v64", "v65", "v66", "v67", "v68", "v69", "v70", "v71", "v72", "v73", "v74", "v75", "v76", "v77", "v78", "v79", "v80", "v81", "v82", "v83", "v84", "v85", "v86", "v87", "v88", "v89", "v90", "v91", "v92", "v93", "v94", "v95", "v96", "v97", "v98", "v99", "v100", "v101", "v102", "v103", "v104", "v105", "v106", "v107", "v108", "v109", "v110", "v111", "v112", "v113", "v114", "v115", "v116", "v117", "v118", "v119", "v120", "v121", "v122", "v123", "v124", "v125", "v126", "v127", "s4", "s5", "s6", "s7", "s8", "s9", "s10", "s11", "s12", "s13", "s14", "s15", "s16", "s17", "s18", "s19", "s20", "s21", "s22", "s23", "s24", "s25", "s26", "s27", "s28", "s29", "s30", "s31", "s34", "s35", "s36", "s37", "s38", "s39", "s40", "s41", "s42", "s43", "s44", "s45", "s46", "s47", "s48", "s49", "s50", "s51", "s52", "s53", "s54", "s55", "s56", "s57", "s58", "s59", "s60", "s61", "s62", "s63", "s64", "s65", "s66", "s67", "s68", "s69", "s70", "s71", "s72", "s73", "s74", "s75", "s76", "s77", "s78", "s79", "s80", "s81", "s82", "s83", "s84", "s85", "s86", "s87", "s88", "s89", "s90", "s91", "s92", "s93", "s94", "s95");
}

extern "C" void kernel_launch(void* const* d_in, const int* in_sizes, int n_in, void* d_out, int out_size, void* d_ws, size_t ws_size,
                              hipStream_t stream) {
  Args a{d_in[0], d_in[1], d_in[2], d_in[3], d_ws, d_in[6], d_in[7], d_in[9], d_in[4], d_in[8], d_out, d_ws};
  fused_decode<<<256, 768, 0, stream>>>(a);
}
```
